```python
import torch

def _softmax(x, axis=-1):
    return torch.softmax(x, dim=axis)
def _gelu(x):
    return torch.nn.functional.gelu(x, approximate="tanh")
def _silu(x):
    return torch.nn.functional.silu(x)
def _sigmoid(x):
    return torch.sigmoid(x)
def _layernorm(x, axis=-1, eps=1e-5):
    m = x.mean(dim=axis, keepdim=True)
    v = x.var(dim=axis, keepdim=True, unbiased=False)
    return (x - m) / torch.sqrt(v + eps)
def _scatter_add(tgt, idx, src, axis=0):
    return tgt.clone().index_add_(axis, idx, src)
def _segment_sum(src, seg, n):
    out = torch.zeros((n,) + src.shape[1:], dtype=src.dtype)
    return out.index_add_(0, seg, src)
def _pad(x, pw):
    flat = tuple(v for pair in reversed(pw) for v in pair)
    return torch.nn.functional.pad(x, flat)

kernel_name = 'interleave_layers__sliding_local_dilated__sliding_local_dilated__prompt'

def setup_inputs():

    _w = (lambda a: (lambda t: t.to(torch.bfloat16) if t.is_floating_point() else t)(torch.as_tensor(a)))
    def _A_setup():
        B, S, D = 1, 2048, 576
        return {
            "x": _w((lambda *s: torch.randn(*s, dtype=torch.bfloat16) * 0.05)(B, S, D)),
            "wqkv": _w((lambda *s: torch.randn(*s, dtype=torch.bfloat16) * 0.05)(D, 3 * D)),
            "wo": _w((lambda *s: torch.randn(*s, dtype=torch.bfloat16) * 0.05)(D, D)),
        }
    _A_in = _A_setup()

    def _B_setup():
        B, S, D = 1, 2048, 576
        return {
            "x": _w((lambda *s: torch.randn(*s, dtype=torch.bfloat16) * 0.05)(B, S, D)),
            "wqkv": _w((lambda *s: torch.randn(*s, dtype=torch.bfloat16) * 0.05)(D, 3 * D)),
            "wo": _w((lambda *s: torch.randn(*s, dtype=torch.bfloat16) * 0.05)(D, D)),
        }
    _B_in = _B_setup()

    return {
        "A_x": _A_in["x"],
        "A_wqkv": _A_in["wqkv"],
        "A_wo": _A_in["wo"],
        "B_x": _B_in["x"],
        "B_wqkv": _B_in["wqkv"],
        "B_wo": _B_in["wo"],
    }

def reference(A_x, A_wqkv, A_wo, B_x, B_wqkv, B_wo):
    def _A_ref(x, wqkv, wo):
        B, S, D = x.shape
        H = 32; Dh = D // H
        W, dil = 8, 2
        qkv = (lambda x, *d: x.permute(*d))((x @ wqkv).reshape(B, S, 3, H, Dh), 2, 0, 3, 1, 4)
        q, k, v = qkv[0], qkv[1], qkv[2]
        scale = Dh ** -0.5
        pos = torch.arange(S)
        off = torch.arange(W) * dil
        idx = pos[:, None] - off[None, :]
        mask = idx >= 0
        idx_c = torch.where(mask, idx, 0).reshape(-1)
        kg = k[:, :, idx_c, :].reshape(B, H, S, W, Dh)
        vg = v[:, :, idx_c, :].reshape(B, H, S, W, Dh)
        logits = torch.einsum("bhsd,bhswd->bhsw", q, kg) * scale
        logits = torch.where(mask[None, None, :, :], logits, -1e9)
        attn = _softmax(logits, axis=-1)
        o = torch.einsum("bhsw,bhswd->bhsd", attn, vg)
        return (lambda x, *d: x.permute(*d))(o, 0, 2, 1, 3).reshape(B, S, D) @ wo

    def _B_ref(x, wqkv, wo):
        B, S, D = x.shape
        H = 32; Dh = D // H
        W, dil = 8, 2
        qkv = (lambda x, *d: x.permute(*d))((x @ wqkv).reshape(B, S, 3, H, Dh), 2, 0, 3, 1, 4)
        q, k, v = qkv[0], qkv[1], qkv[2]
        scale = Dh ** -0.5
        pos = torch.arange(S)
        off = torch.arange(W) * dil
        idx = pos[:, None] - off[None, :]
        mask = idx >= 0
        idx_c = torch.where(mask, idx, 0).reshape(-1)
        kg = k[:, :, idx_c, :].reshape(B, H, S, W, Dh)
        vg = v[:, :, idx_c, :].reshape(B, H, S, W, Dh)
        logits = torch.einsum("bhsd,bhswd->bhsw", q, kg) * scale
        logits = torch.where(mask[None, None, :, :], logits, -1e9)
        attn = _softmax(logits, axis=-1)
        o = torch.einsum("bhsw,bhswd->bhsd", attn, vg)
        return (lambda x, *d: x.permute(*d))(o, 0, 2, 1, 3).reshape(B, S, D) @ wo

    x = A_x
    x = x + _A_ref(_layernorm(x, axis=-1), A_wqkv, A_wo)
    x = x + _B_ref(_layernorm(x, axis=-1), B_wqkv, B_wo)
    x = x + _A_ref(_layernorm(x, axis=-1), A_wqkv, A_wo)
    x = x + _B_ref(_layernorm(x, axis=-1), B_wqkv, B_wo)
    return x
```

```cpp
#include <hip/hip_runtime.h>
#include <hip/hip_bf16.h>
#include <stdint.h>

#define S_ 2048
#define D_ 576
#define H_ 32
#define DH 18

__device__ __forceinline__ float bf2f(uint16_t v){ return __uint_as_float(((uint32_t)v)<<16); }
__device__ __forceinline__ uint16_t f2bf(float f){ __hip_bfloat16 b = __float2bfloat16(f); return *(uint16_t*)&b; }

__global__ void k_ln(const uint16_t* x, uint16_t* y){
  int r = blockIdx.x; int t = threadIdx.x;
  float v[9]; float s=0;
  for(int i=0;i<9;i++){ v[i]=bf2f(x[r*D_+t+64*i]); s+=v[i]; }
  for(int o=32;o;o>>=1) s+=__shfl_xor(s,o);
  float m=s/D_; float q=0;
  for(int i=0;i<9;i++){ float d=v[i]-m; q+=d*d; }
  for(int o=32;o;o>>=1) q+=__shfl_xor(q,o);
  float rs=rsqrtf(q/D_+1e-5f);
  for(int i=0;i<9;i++) y[r*D_+t+64*i]=f2bf((v[i]-m)*rs);
}
__global__ void k_gemm(const uint16_t* A,const uint16_t* B,const uint16_t* res,uint16_t* C,int M,int N,int K){
  __shared__ float As[16][65]; __shared__ float Bs[64][64];
  int n0=blockIdx.x*64, m0=blockIdx.y*16; int tx=threadIdx.x&63, ty=threadIdx.x>>6;
  float acc[4]={0,0,0,0};
  for(int k0=0;k0<K;k0+=64){
    for(int i=threadIdx.x;i<16*64;i+=256) As[i>>6][i&63]=bf2f(A[(m0+(i>>6))*K+k0+(i&63)]);
    for(int i=threadIdx.x;i<64*64;i+=256) Bs[i>>6][i&63]=bf2f(B[(k0+(i>>6))*N+n0+(i&63)]);
    __syncthreads();
    for(int k=0;k<64;k++){ float b=Bs[k][tx]; for(int j=0;j<4;j++) acc[j]+=As[ty*4+j][k]*b; }
    __syncthreads();
  }
  for(int j=0;j<4;j++){ int m=m0+ty*4+j; float v=acc[j]; if(res){ v=bf2f(f2bf(v))+bf2f(res[m*N+n0+tx]); } C[m*N+n0+tx]=f2bf(v);}
}
__global__ void k_attn(const uint16_t* qkv, uint16_t* o){
  int idx=blockIdx.x*blockDim.x+threadIdx.x; int h=idx&31, s=idx>>5;
  float q[DH]; for(int d=0;d<DH;d++) q[d]=bf2f(qkv[s*3*D_+h*DH+d]);
  float l[8]; float mx=-1e30f;
  for(int w=0;w<8;w++){ int p=s-2*w; if(p>=0){ float a=0; for(int d=0;d<DH;d++) a+=q[d]*bf2f(qkv[p*3*D_+D_+h*DH+d]); l[w]=a*0.23570226f; mx=fmaxf(mx,l[w]); } else l[w]=-1e30f; }
  float sum=0; for(int w=0;w<8;w++){ l[w]=(s-2*w>=0)?__expf(l[w]-mx):0.f; sum+=l[w]; }
  float inv=1.f/sum;
  for(int d=0;d<DH;d++){ float a=0; for(int w=0;w<8;w++){ int p=s-2*w; if(p>=0) a+=l[w]*inv*bf2f(qkv[p*3*D_+2*D_+h*DH+d]); } o[s*D_+h*DH+d]=f2bf(a);}
}
extern "C" void kernel_launch(void* const* d_in, const int* in_sizes, int n_in, void* d_out, int out_size, void* d_ws, size_t ws_size, hipStream_t stream){
  const uint16_t* x0=(const uint16_t*)d_in[0];
  const uint16_t* wq[2]={(const uint16_t*)d_in[1],(const uint16_t*)d_in[4]};
  const uint16_t* wo[2]={(const uint16_t*)d_in[2],(const uint16_t*)d_in[5]};
  uint16_t* ws=(uint16_t*)d_ws;
  uint16_t* ln=ws; uint16_t* qkv=ln+S_*D_; uint16_t* o=qkv+S_*3*D_; uint16_t* xa=o+S_*D_; uint16_t* xb=xa+S_*D_;
  const uint16_t* x=x0;
  for(int l=0;l<4;l++){
    uint16_t* xn=(l==3)?(uint16_t*)d_out:((l&1)?xb:xa);
    k_ln<<<S_,64,0,stream>>>(x,ln);
    k_gemm<<<dim3(27,128),256,0,stream>>>(ln,wq[l&1],nullptr,qkv,S_,3*D_,D_);
    k_attn<<<S_*H_/256,256,0,stream>>>(qkv,o);
    k_gemm<<<dim3(9,128),256,0,stream>>>(o,wo[l&1],x,xn,S_,D_,D_);
    x=xn;
  }
}
```

```cpp
#include <hip/hip_runtime.h>
#include <stdint.h>

typedef __attribute__((ext_vector_type(4))) unsigned u32x4;
typedef __attribute__((ext_vector_type(2))) unsigned u32x2;
#define LAS __attribute__((address_space(3)))
constexpr unsigned OT_B = 18432u;
constexpr unsigned XT_B = 20480u;
constexpr unsigned KV_B = 4096u;
constexpr unsigned BLK_X = 147456u;
constexpr unsigned BLK_KV = 311296u;
constexpr unsigned BLK_B = 376832u;
constexpr unsigned GROUP_B = 1507328u;
constexpr unsigned WQ_OFF = 65536u;
constexpr unsigned WO_OFF = 4194304u;
constexpr unsigned ACT_OFF = 6291456u;
constexpr unsigned L_Q = 129024u;
constexpr unsigned QP = 4608u;
constexpr unsigned L_FL = 162176u;
constexpr unsigned LDS_BYTES = 162880u;
constexpr unsigned DBG_OFF = 67108864u;
constexpr unsigned POISON = 0xFFFFFFFFu;
__device__ __forceinline__ unsigned nopoison(unsigned v) { return v < 0xFFFFFFFEu ? v : 0xFFFFFFFEu; }

constexpr int NCH_WQ = 2 * 16 * 7 * 18 * 64, NCH_WO = 2 * 36 * 18 * 64, NCH_X = 128 * 18 * 64, NCH_ST = 2048 * 64;
constexpr int CLR_PER = (16 * 2048 + 16 * KV_B) / 16;
constexpr int NCH_CLR = 64 * CLR_PER, NCH_CNT = 0;
constexpr int NCH_ALL = NCH_WQ + NCH_WO + NCH_X + NCH_ST + NCH_CLR + NCH_CNT;

__global__ void __launch_bounds__(256) prep_kernel(const uint16_t* __restrict__ x0, const uint16_t* __restrict__ wqA, const uint16_t* __restrict__ woA,
                                                   const uint16_t* __restrict__ wqB, const uint16_t* __restrict__ woB, char* ws) {
  int c = blockIdx.x * 256 + threadIdx.x;
  if (c < NCH_WQ) {
    const int lane = c & 63; int r = c >> 6;
    const int s = r % 18; r /= 18; const int tile = r % 7; r /= 7; const int j = r & 15, type = r >> 4;
    const uint16_t* W = type ? wqB : wqA;
    const int n = lane & 15, g = lane >> 4;
    int col; bool pad = false;
    if (tile < 6) { const int part = tile < 4 ? (tile & 1) : 2; const int odd = tile < 4 ? (tile >> 1) : (tile & 1); col = part * 576 + 36 * j + odd * 20 + n; }
    else { pad = n >= 12; col = (pad ? 0 : (n >> 2)) * 576 + 36 * j + 16 + (n & 3); }
    unsigned e[8];
#pragma unroll
    for (int q = 0; q < 8; ++q) { const int k = s < 16 ? 32 * s + 16 * (q >> 2) + 4 * g + (q & 3) : 32 * s + 16 * (g >> 1) + 4 * (g & 1) + 8 * (q >> 2) + (q & 3); e[q] = pad ? 0u : (unsigned)W[k * 1728 + col]; }
    u32x4 v = {e[0] | (e[1] << 16), e[2] | (e[3] << 16), e[4] | (e[5] << 16), e[6] | (e[7] << 16)};
    *(u32x4*)(ws + WQ_OFF + (size_t)c * 16) = v;
    return;
  }
  c -= NCH_WQ;
  if (c < NCH_WO) {
    const int lane = c & 63; int r = c >> 6;
    const int s = r % 18; r /= 18; const int ct = r % 36, type = r / 36;
    const uint16_t* W = type ? woB : woA;
    const int n = lane & 15, g = lane >> 4;
    unsigned e[8];
#pragma unroll
    for (int q = 0; q < 8; ++q) {
      const int half = q >> 2, qq = q & 3;
      const int f = s < 16 ? 36 * s + (g >> 1) * 20 + 4 * (g & 1) + 8 * half + qq : 36 * (8 * (s - 16) + 2 * g + half) + 16 + qq;
      e[q] = W[f * 576 + 16 * ct + n];
    }
    u32x4 v = {e[0] | (e[1] << 16), e[2] | (e[3] << 16), e[4] | (e[5] << 16), e[6] | (e[7] << 16)};
    *(u32x4*)(ws + WO_OFF + (size_t)c * 16) = v;
    return;
  }
  c -= NCH_WO;
  if (c < NCH_X) {
    const int lane = c & 63; int r = c >> 6;
    const int s = r % 18; r /= 18; const int t = r & 7, grp = r >> 3;
    const int tok = lane & 15, g = lane >> 4;
    const int row = 2 * (128 * (grp & 7) + 16 * t + tok) + (grp >> 3);
    const int c0 = s < 16 ? 32 * s + 4 * g : 32 * s + 16 * (g >> 1) + 4 * (g & 1), c1 = c0 + (s < 16 ? 16 : 8);
    const u32x2 a = *(const u32x2*)(x0 + row * 576 + c0), b = *(const u32x2*)(x0 + row * 576 + c1);
    u32x4 v = {a.x, a.y, b.x, b.y};
    *(u32x4*)(ws + ACT_OFF + (size_t)grp * GROUP_B + BLK_X + t * XT_B + s * 1024 + lane * 16) = v;
    return;
  }
  c -= NCH_X;
  if (c < NCH_ST) {
    const int lane = c & 63, r = c >> 6;
    const int tok = r & 15, t = (r >> 4) & 7, grp = r >> 7;
    const int row = 2 * (128 * (grp & 7) + 16 * t + tok) + (grp >> 3);
    float s = 0, q = 0;
#pragma unroll
    for (int i = 0; i < 9; ++i) { const float v = __uint_as_float((unsigned)x0[row * 576 + lane + 64 * i] << 16); s += v; q += v * v; }
#pragma unroll
    for (int o = 32; o; o >>= 1) { s += __shfl_xor(s, o); q += __shfl_xor(q, o); }
    if (lane < 16) {
      u32x2 v = {lane == 0 ? nopoison(__float_as_uint(s)) : 0u, lane == 0 ? nopoison(__float_as_uint(q)) : 0u};
      *(u32x2*)(ws + ACT_OFF + (size_t)grp * GROUP_B + BLK_X + t * XT_B + 18432 + lane * 128 + tok * 8) = v;
    }
    return;
  }
  c -= NCH_ST;
  if (c < NCH_CLR) {
    const int gl = c / CLR_PER; const unsigned o = (c % CLR_PER) * 16, i = o >> 11;
    const unsigned off = i < 8 ? i * OT_B + 16384 + (o & 2047) : i < 16 ? BLK_X + (i - 8) * XT_B + 18432 + (o & 2047) : BLK_KV + (o - 32768);
    u32x4 v = {POISON, POISON, POISON, POISON};
    if (!((gl & 3) == 0 && i >= 8 && i < 16)) *(u32x4*)(ws + ACT_OFF + (size_t)gl * BLK_B + off) = v;
  }
}
__global__ void __launch_bounds__(512, 2) main_kernel(uint16_t* __restrict__ out, char* ws) {
  extern __shared__ __attribute__((aligned(16))) char lds_raw[];
  LAS char* L = (LAS char*)lds_raw;
  const int wave = __builtin_amdgcn_readfirstlane(threadIdx.x >> 6);
  if (threadIdx.x < 48) {
    const unsigned blk = threadIdx.x >> 3, i = threadIdx.x & 7;
    unsigned v = i == 5 ? 0x100u : (i == 6 && blk > 2) ? 1u : 0u;
    if (blk == 0) v = i < 2 ? 0x44444444u : i == 5 ? 0x1FFu : 0u;
    if (blk == 1 && i == 2) v = 255u;
    *(LAS unsigned*)(L + L_FL + threadIdx.x * 4) = v;
  }
  for (unsigned i = threadIdx.x; i < (6 * QP + 3456) / 16; i += 512) *(LAS u32x4*)(L + L_Q + i * 16) = (u32x4){0, 0, 0, 0};
  __syncthreads();
  const unsigned long long pw = (unsigned long long)ws, po = (unsigned long long)out;
  asm volatile(
      "s_mov_b32 s8, %0\n"
      "s_and_b32 s9, %1, 0xffff\n"
      "s_mov_b32 s10, 30408704\n"
      "s_mov_b32 s11, 0x20000\n"
      "s_mov_b32 s12, %2\n"
      "s_and_b32 s13, %3, 0xffff\n"
      "s_mov_b32 s14, 2359296\n"
      "s_mov_b32 s15, 0x20000\n"
      "s_and_b32 s17, %5, 3\n"
      "s_lshr_b32 s23, %5, 2\n"
      "s_and_b32 s19, %4, 7\n"
      "s_lshl_b32 s19, s19, 1\n"
      "s_lshr_b32 s28, %4, 7\n"
      "s_add_u32 s19, s19, s28\n"
      "s_lshr_b32 s18, %4, 3\n"
      "s_and_b32 s18, s18, 15\n"
      "s_mov_b32 s21, 0x100000\n"
      "s_mov_b32 s16, 0\n"
      "s_mov_b32 s65, 0x300\n"
      "s_mul_i32 s20, s19, 1507328\n"
      "s_add_u32 s20, s20, 6291456\n"
      "s_mul_i32 s24, s18, 7\n"
      "s_lshl_b32 s28, s17, 1\n"
      "s_add_u32 s24, s24, s28\n"
      "s_mul_i32 s24, s24, 18432\n"
      "s_mul_i32 s28, s23, 2064384\n"
      "s_add_u32 s24, s24, s28\n"
      "s_add_u32 s24, s24, 65536\n"
      "v_mbcnt_lo_u32_b32 v17, -1, 0\n"
      "v_mbcnt_hi_u32_b32 v17, -1, v17\n"
      "v_lshlrev_b32 v0, 4, v17\n"
      "v_and_b32 v18, 15, v17\n"
      "v_lshrrev_b32 v19, 4, v17\n"
      "v_lshlrev_b32 v1, 2, v17\n"
      "v_add_u32 v1, 162368, v1\n"
      "v_mov_b32 v2, 162176\n"
      "v_cmp_eq_u32 vcc, 0, v17\n"
      "s_nop 1\n"
      "v_cndmask_b32 v1, v1, v2, vcc\n"
      "v_cmp_gt_u32 s[70:71], 8, v18\n"
      "s_mov_b64 s[72:73], 0xff0000\n"
      "s_mov_b32 s74, 0xff000000\n"
      "s_mov_b32 s75, 0\n"
      "v_cmp_gt_u32 s[76:77], 2, v18\n"
      "v_cmp_gt_u32 s[78:79], 4, v18\n"
      "s_nop 1\n"
      "s_andn2_b64 s[78:79], s[78:79], s[76:77]\n"
      "v_lshlrev_b32 v20, 3, v18\n"
      "v_add_u32 v4, 160128, v20\n"
      "v_mul_u32_u24 v21, 1152, v19\n"
      "v_add_u32 v3, v21, v20\n"
      "v_add_u32 v3, 128, v3\n"
      "v_lshlrev_b32 v14, 3, v17\n"
      "s_mov_b32 s84, 7\n"
      "s_cmp_lg_u32 s17, 2\n"
      "s_cbranch_scc1 P1_%=\n"
      "v_lshlrev_b32 v21, 5, v18\n"
      "v_lshl_add_u32 v14, v19, 3, v21\n"
      "v_add_u32 v3, 512, v14\n"
      "s_mov_b32 s84, 9\n"
      "P1_%=:\n"
      "s_mul_i32 s28, s17, 9216\n"
      "s_add_u32 s28, s28, 129024\n"
      "v_add_u32 v3, s28, v3\n"
      "v_mov_b32 v5, 1.0\n"
      "v_mov_b32 v20, 0x3eae1a93\n"
      "s_cmp_lt_u32 s17, 2\n"
      "s_cselect_b64 s[28:29], -1, 0\n"
      "s_cmp_eq_u32 s17, 3\n"
      "s_cselect_b64 s[28:29], 0xffff, s[28:29]\n"
      "v_cndmask_b32 v5, v5, v20, s[28:29]\n"
      "v_add_u32 v15, 36864, v0\n"
      "v_lshlrev_b32 v16, 3, v17\n"
      "v_mov_b32 v20, 0x80000000\n"
      "v_cmp_gt_u32 vcc, 16, v17\n"
      "s_nop 1\n"
      "v_cndmask_b32 v16, v20, v16, vcc\n"
      "LAYER_%=:\n"
      "s_add_u32 s22, s16, 4\n"
      "s_lshl_b32 s94, s16, 3\n"
      "s_and_b32 s28, s16, 1\n"
      "s_cmp_lg_u32 s28, s23\n"
      "s_cbranch_scc1 HELP_%=\n"
      "s_or_b32 s28, s17, s16\n"
      "s_cmp_eq_u32 s16, 0\n"
      "s_cselect_b32 s28, 1, s17\n"
      "s_cmp_lg_u32 s28, 0\n"
      "s_cbranch_scc1 G_ROLE_%=\n"
      "s_mov_b32 s26, 0\n"
      "s_mov_b32 s89, 1\n"
      "s_branch S_ROLE_%=\n"
      "G_ROLE_%=:\n"
      "s_setprio 0\n"
      "s_cmp_gt_u32 s16, 1\n"
      "s_cbranch_scc1 G_GO_%=\n"
      "s_cmp_lg_u32 s16, 0\n"
      "s_cbranch_scc1 G_CS_%=\n"
      "s_cmp_eq_u32 s17, 3\n"
      "s_cbranch_scc1 G_W3_%=\n"
      "s_add_u32 s28, s24, 0\n"
      "s_add_u32 s29, s24, 18432\n"
      "buffer_load_dwordx4 v[112:115], v0, s[8:11], s28 offen offset:0\n"
      "buffer_load_dwordx4 v[116:119], v0, s[8:11], s29 offen offset:0\n"
      "buffer_load_dwordx4 v[120:123], v0, s[8:11], s28 offen offset:1024\n"
      "buffer_load_dwordx4 v[124:127], v0, s[8:11], s29 offen offset:1024\n"
      "buffer_load_dwordx4 v[128:131], v0, s[8:11], s28 offen offset:2048\n"
      "buffer_load_dwordx4 v[132:135], v0, s[8:11], s29 offen offset:2048\n"
      "buffer_load_dwordx4 v[136:139], v0, s[8:11], s28 offen offset:3072\n"
      "buffer_load_dwordx4 v[140:143], v0, s[8:11], s29 offen offset:3072\n"
      "s_add_u32 s28, s24, 4096\n"
      "s_add_u32 s29, s24, 22528\n"
      "buffer_load_dwordx4 v[144:147], v0, s[8:11], s28 offen offset:0\n"
      "buffer_load_dwordx4 v[148:151], v0, s[8:11], s29 offen offset:0\n"
      "buffer_load_dwordx4 v[152:155], v0, s[8:11], s28 offen offset:1024\n"
      "buffer_load_dwordx4 v[156:159], v0, s[8:11], s29 offen offset:1024\n"
      "buffer_load_dwordx4 v[160:163], v0, s[8:11], s28 offen offset:2048\n"
      "buffer_load_dwordx4 v[164:167], v0, s[8:11], s29 offen offset:2048\n"
      "buffer_load_dwordx4 v[168:171], v0, s[8:11], s28 offen offset:3072\n"
      "buffer_load_dwordx4 v[172:175], v0, s[8:11], s29 offen offset:3072\n"
      "s_add_u32 s28, s24, 8192\n"
      "s_add_u32 s29, s24, 26624\n"
      "buffer_load_dwordx4 v[176:179], v0, s[8:11], s28 offen offset:0\n"
      "buffer_load_dwordx4 v[180:183], v0, s[8:11], s29 offen offset:0\n"
      "buffer_load_dwordx4 v[184:187], v0, s[8:11], s28 offen offset:1024\n"
      "buffer_load_dwordx4 v[188:191], v0, s[8:11], s29 offen offset:1024\n"
      "buffer_load_dwordx4 v[192:195], v0, s[8:11], s28 offen offset:2048\n"
      "buffer_load_dwordx4 v[196:199], v0, s[8:11], s29 offen offset:2048\n"
      "buffer_load_dwordx4 v[200:203], v0, s[8:11], s28 offen offset:3072\n"
      "buffer_load_dwordx4 v[204:207], v0, s[8:11], s29 offen offset:3072\n"
      "s_add_u32 s28, s24, 12288\n"
      "s_add_u32 s29, s24, 30720\n"
      "buffer_load_dwordx4 v[208:211], v0, s[8:11], s28 offen offset:0\n"
      "buffer_load_dwordx4 v[212:215], v0, s[8:11], s29 offen offset:0\n"
      "buffer_load_dwordx4 v[216:219], v0, s[8:11], s28 offen offset:1024\n"
      "buffer_load_dwordx4 v[220:223], v0, s[8:11], s29 offen offset:1024\n"
      "buffer_load_dwordx4 v[224:227], v0, s[8:11], s28 offen offset:2048\n"
      "buffer_load_dwordx4 v[228:231], v0, s[8:11], s29 offen offset:2048\n"
      "buffer_load_dwordx4 v[232:235], v0, s[8:11], s28 offen offset:3072\n"
      "buffer_load_dwordx4 v[236:239], v0, s[8:11], s29 offen offset:3072\n"
      "s_add_u32 s28, s24, 16384\n"
      "s_add_u32 s29, s24, 34816\n"
      "buffer_load_dwordx4 v[240:243], v0, s[8:11], s28 offen offset:0\n"
      "buffer_load_dwordx4 v[244:247], v0, s[8:11], s29 offen offset:0\n"
      "buffer_load_dwordx4 v[248:251], v0, s[8:11], s28 offen offset:1024\n"
      "buffer_load_dwordx4 v[252:255], v0, s[8:11], s29 offen offset:1024\n"
      "s_branch G_CS_%=\n"
      "G_W3_%=:\n"
      "s_add_u32 s28, s24, 0\n"
      "buffer_load_dwordx4 v[112:115], v0, s[8:11], s28 offen offset:0\n"
      "buffer_load_dwordx4 v[120:123], v0, s[8:11], s28 offen offset:1024\n"
      "buffer_load_dwordx4 v[128:131], v0, s[8:11], s28 offen offset:2048\n"
      "buffer_load_dwordx4 v[136:139], v0, s[8:11], s28 offen offset:3072\n"
      "s_add_u32 s28, s24, 4096\n"
      "buffer_load_dwordx4 v[144:147], v0, s[8:11], s28 offen offset:0\n"
      "buffer_load_dwordx4 v[152:155], v0, s[8:11], s28 offen offset:1024\n"
      "buffer_load_dwordx4 v[160:163], v0, s[8:11], s28 offen offset:2048\n"
      "buffer_load_dwordx4 v[168:171], v0, s[8:11], s28 offen offset:3072\n"
      "s_add_u32 s28, s24, 8192\n"
      "buffer_load_dwordx4 v[176:179], v0, s[8:11], s28 offen offset:0\n"
      "buffer_load_dwordx4 v[184:187], v0, s[8:11], s28 offen offset:1024\n"
      "buffer_load_dwordx4 v[192:195], v0, s[8:11], s28 offen offset:2048\n"
      "buffer_load_dwordx4 v[200:203], v0, s[8:11], s28 offen offset:3072\n"
      "s_add_u32 s28, s24, 12288\n"
      "buffer_load_dwordx4 v[208:211], v0, s[8:11], s28 offen offset:0\n"
      "buffer_load_dwordx4 v[216:219], v0, s[8:11], s28 offen offset:1024\n"
      "buffer_load_dwordx4 v[224:227], v0, s[8:11], s28 offen offset:2048\n"
      "buffer_load_dwordx4 v[232:235], v0, s[8:11], s28 offen offset:3072\n"
      "s_add_u32 s28, s24, 16384\n"
      "buffer_load_dwordx4 v[240:243], v0, s[8:11], s28 offen offset:0\n"
      "buffer_load_dwordx4 v[248:251], v0, s[8:11], s28 offen offset:1024\n"
      "G_CS_%=:\n"
      "v_mov_b32 v20, 0x3f803f80\n"
      "v_mov_b32 v21, 0x3f803f80\n"
      "v_mov_b32 v22, 0x3f803f80\n"
      "v_mov_b32 v23, 0x3f803f80\n"
      "s_waitcnt vmcnt(0)\n"
      "s_nop 0\n"
      "v_mfma_f32_16x16x32_bf16 v[6:9], v[112:115], v[20:23], 0\n"
      "v_mfma_f32_16x16x32_bf16 v[10:13], v[116:119], v[20:23], 0\n"
      "v_mfma_f32_16x16x32_bf16 v[6:9], v[120:123], v[20:23], v[6:9]\n"
      "v_mfma_f32_16x16x32_bf16 v[10:13], v[124:127], v[20:23], v[10:13]\n"
      "v_mfma_f32_16x16x32_bf16 v[6:9], v[128:131], v[20:23], v[6:9]\n"
      "v_mfma_f32_16x16x32_bf16 v[10:13], v[132:135], v[20:23], v[10:13]\n"
      "v_mfma_f32_16x16x32_bf16 v[6:9], v[136:139], v[20:23], v[6:9]\n"
      "v_mfma_f32_16x16x32_bf16 v[10:13], v[140:143], v[20:23], v[10:13]\n"
      "v_mfma_f32_16x16x32_bf16 v[6:9], v[144:147], v[20:23], v[6:9]\n"
      "v_mfma_f32_16x16x32_bf16 v[10:13], v[148:151], v[20:23], v[10:13]\n"
      "v_mfma_f32_16x16x32_bf16 v[6:9], v[152:155], v[20:23], v[6:9]\n"
      "v_mfma_f32_16x16x32_bf16 v[10:13], v[156:159], v[20:23], v[10:13]\n"
      "v_mfma_f32_16x16x32_bf16 v[6:9], v[160:163], v[20:23], v[6:9]\n"
      "v_mfma_f32_16x16x32_bf16 v[10:13], v[164:167], v[20:23], v[10:13]\n"
      "v_mfma_f32_16x16x32_bf16 v[6:9], v[168:171], v[20:23], v[6:9]\n"
      "v_mfma_f32_16x16x32_bf16 v[10:13], v[172:175], v[20:23], v[10:13]\n"
      "v_mfma_f32_16x16x32_bf16 v[6:9], v[176:179], v[20:23], v[6:9]\n"
      "v_mfma_f32_16x16x32_bf16 v[10:13], v[180:183], v[20:23], v[10:13]\n"
      "v_mfma_f32_16x16x32_bf16 v[6:9], v[184:187], v[20:23], v[6:9]\n"
      "v_mfma_f32_16x16x32_bf16 v[10:13], v[188:191], v[20:23], v[10:13]\n"
      "v_mfma_f32_16x16x32_bf16 v[6:9], v[192:195], v[20:23], v[6:9]\n"
      "v_mfma_f32_16x16x32_bf16 v[10:13], v[196:199], v[20:23], v[10:13]\n"
      "v_mfma_f32_16x16x32_bf16 v[6:9], v[200:203], v[20:23], v[6:9]\n"
      "v_mfma_f32_16x16x32_bf16 v[10:13], v[204:207], v[20:23], v[10:13]\n"
      "v_mfma_f32_16x16x32_bf16 v[6:9], v[208:211], v[20:23], v[6:9]\n"
      "v_mfma_f32_16x16x32_bf16 v[10:13], v[212:215], v[20:23], v[10:13]\n"
      "v_mfma_f32_16x16x32_bf16 v[6:9], v[216:219], v[20:23], v[6:9]\n"
      "v_mfma_f32_16x16x32_bf16 v[10:13], v[220:223], v[20:23], v[10:13]\n"
      "v_mfma_f32_16x16x32_bf16 v[6:9], v[224:227], v[20:23], v[6:9]\n"
      "v_mfma_f32_16x16x32_bf16 v[10:13], v[228:231], v[20:23], v[10:13]\n"
      "v_mfma_f32_16x16x32_bf16 v[6:9], v[232:235], v[20:23], v[6:9]\n"
      "v_mfma_f32_16x16x32_bf16 v[10:13], v[236:239], v[20:23], v[10:13]\n"
      "v_mfma_f32_16x16x32_bf16 v[6:9], v[240:243], v[20:23], v[6:9]\n"
      "v_mfma_f32_16x16x32_bf16 v[10:13], v[244:247], v[20:23], v[10:13]\n"
      "v_mfma_f32_16x16x32_bf16 v[6:9], v[248:251], v[20:23], v[6:9]\n"
      "v_mfma_f32_16x16x32_bf16 v[10:13], v[252:255], v[20:23], v[10:13]\n"
      "G_GO_%=:\n"
      "v_mov_b32 v18, v0\n"
      "s_and_b32 s28, s16, 1\n"
      "s_lshl_b32 s28, s28, 10\n"
      "v_add_u32 v24, s28, v4\n"
      "s_and_b32 s31, s22, 7\n"
      "s_mov_b32 s26, 0\n"
      "v_mov_b32 v25, 1\n"
      "s_mov_b32 s30, 18432\n"
      "s_cmp_eq_u32 s17, 3\n"
      "s_cbranch_scc1 G3_PRE_%=\n"
      "G_TILE_%=:\n"
      "ds_read_b32 v96, v2 offset:44\n"
      "ds_read_b64 v[98:99], v24\n"
      "ds_read_b128 v[64:67], v18 offset:0\n"
      "ds_read_b128 v[68:71], v18 offset:1024\n"
      "ds_read_b128 v[72:75], v18 offset:2048\n"
      "ds_read_b128 v[76:79], v18 offset:3072\n"
      "ds_read_b128 v[80:83], v18 offset:4096\n"
      "s_lshl_b32 s28, s31, s84\n"
      "v_add_u32 v23, s28, v3\n"
      "s_lshl_b32 s28, s31, 2\n"
      "s_lshl_b32 s29, 1, s28\n"
      "v_mov_b32 v26, s29\n"
      "s_waitcnt lgkmcnt(5)\n"
      "v_readfirstlane_b32 s27, v96\n"
      "v_mul_f32 v88, v98, v6\n"
      "v_mul_f32 v89, v98, v7\n"
      "v_mul_f32 v90, v98, v8\n"
      "v_mul_f32 v91, v98, v9\n"
      "v_mul_f32 v92, v98, v10\n"
      "v_mul_f32 v93, v98, v11\n"
      "v_mul_f32 v94, v98, v12\n"
      "v_mul_f32 v95, v98, v13\n"
      "s_bitcmp1_b32 s27, s26\n"
      "s_cbranch_scc0 G_XW_%=\n"
      "s_waitcnt lgkmcnt(3)\n"
      "v_mfma_f32_16x16x32_bf16 v[88:91], v[112:115], v[64:67], v[88:91]\n"
      "v_mfma_f32_16x16x32_bf16 v[92:95], v[116:119], v[64:67], v[92:95]\n"
      "ds_read_b128 v[84:87], v18 offset:5120\n"
      "v_mul_f32 v20, v99, v5\n"
      "v_mfma_f32_16x16x32_bf16 v[88:91], v[120:123], v[68:71], v[88:91]\n"
      "v_mfma_f32_16x16x32_bf16 v[92:95], v[124:127], v[68:71], v[92:95]\n"
      "ds_read_b128 v[64:67], v18 offset:6144\n"
      "s_waitcnt lgkmcnt(3)\n"
      "v_mfma_f32_16x16x32_bf16 v[88:91], v[128:131], v[72:75], v[88:91]\n"
      "v_mfma_f32_16x16x32_bf16 v[92:95], v[132:135], v[72:75], v[92:95]\n"
      "ds_read_b128 v[68:71], v18 offset:7168\n"
      "v_mfma_f32_16x16x32_bf16 v[88:91], v[136:139], v[76:79], v[88:91]\n"
      "v_mfma_f32_16x16x32_bf16 v[92:95], v[140:143], v[76:79], v[92:95]\n"
      "ds_read_b128 v[72:75], v18 offset:8192\n"
      "ds_add_u32 v1, v25 offset:32\n"
      "s_waitcnt lgkmcnt(4)\n"
      "v_mfma_f32_16x16x32_bf16 v[88:91], v[144:147], v[80:83], v[88:91]\n"
      "v_mfma_f32_16x16x32_bf16 v[92:95], v[148:151], v[80:83], v[92:95]\n"
      "ds_read_b128 v[76:79], v18 offset:9216\n"
      "v_mfma_f32_16x16x32_bf16 v[88:91], v[152:155], v[84:87], v[88:91]\n"
      "v_mfma_f32_16x16x32_bf16 v[92:95], v[156:159], v[84:87], v[92:95]\n"
      "ds_read_b128 v[80:83], v18 offset:10240\n"
      "s_waitcnt lgkmcnt(4)\n"
      "v_mfma_f32_16x16x32_bf16 v[88:91], v[160:163], v[64:67], v[88:91]\n"
      "v_mfma_f32_16x16x32_bf16 v[92:95], v[164:167], v[64:67], v[92:95]\n"
      "ds_read_b128 v[84:87], v18 offset:11264\n"
      "v_mfma_f32_16x16x32_bf16 v[88:91], v[168:171], v[68:71], v[88:91]\n"
      "v_mfma_f32_16x16x32_bf16 v[92:95], v[172:175], v[68:71], v[92:95]\n"
      "ds_read_b128 v[64:67], v18 offset:12288\n"
      "s_waitcnt lgkmcnt(3)\n"
      "v_mfma_f32_16x16x32_bf16 v[88:91], v[176:179], v[72:75], v[88:91]\n"
      "v_mfma_f32_16x16x32_bf16 v[92:95], v[180:183], v[72:75], v[92:95]\n"
      "ds_read_b128 v[68:71], v18 offset:13312\n"
      "v_mfma_f32_16x16x32_bf16 v[88:91], v[184:187], v[76:79], v[88:91]\n"
      "v_mfma_f32_16x16x32_bf16 v[92:95], v[188:191], v[76:79], v[92:95]\n"
      "ds_read_b128 v[72:75], v18 offset:14336\n"
      "s_waitcnt lgkmcnt(3)\n"
      "v_mfma_f32_16x16x32_bf16 v[88:91], v[192:195], v[80:83], v[88:91]\n"
      "v_mfma_f32_16x16x32_bf16 v[92:95], v[196:199], v[80:83], v[92:95]\n"
      "ds_read_b128 v[76:79], v18 offset:15360\n"
      "ds_read_b32 v97, v2 offset:20\n"
      "v_mfma_f32_16x16x32_bf16 v[88:91], v[200:203], v[84:87], v[88:91]\n"
      "v_mfma_f32_16x16x32_bf16 v[92:95], v[204:207], v[84:87], v[92:95]\n"
      "ds_read_b128 v[80:83], v18 offset:16384\n"
      "s_waitcnt lgkmcnt(4)\n"
      "v_mfma_f32_16x16x32_bf16 v[88:91], v[208:211], v[64:67], v[88:91]\n"
      "v_mfma_f32_16x16x32_bf16 v[92:95], v[212:215], v[64:67], v[92:95]\n"
      "ds_read_b128 v[84:87], v18 offset:17408\n"
      "ds_add_u32 v1, v25 offset:36\n"
      "v_mfma_f32_16x16x32_bf16 v[88:91], v[216:219], v[68:71], v[88:91]\n"
      "v_add_u32 v18, s30, v18\n"
      "v_mfma_f32_16x16x32_bf16 v[92:95], v[220:223], v[68:71], v[92:95]\n"
      "s_sub_u32 s30, 0, s30\n"
      "s_waitcnt lgkmcnt(3)\n"
      "v_mfma_f32_16x16x32_bf16 v[88:91], v[224:227], v[72:75], v[88:91]\n"
      "v_lshlrev_b32 v25, 4, v25\n"
      "v_mfma_f32_16x16x32_bf16 v[92:95], v[228:231], v[72:75], v[92:95]\n"
      "v_add_u32 v24, 128, v24\n"
      "v_mfma_f32_16x16x32_bf16 v[88:91], v[232:235], v[76:79], v[88:91]\n"
      "v_readfirstlane_b32 s27, v97\n"
      "v_mfma_f32_16x16x32_bf16 v[92:95], v[236:239], v[76:79], v[92:95]\n"
      "s_lshl_b32 s35, 3, s31\n"
      "s_waitcnt lgkmcnt(1)\n"
      "v_mfma_f32_16x16x32_bf16 v[88:91], v[240:243], v[80:83], v[88:91]\n"
      "s_add_u32 s26, s26, 1\n"
      "v_mfma_f32_16x16x32_bf16 v[92:95], v[244:247], v[80:83], v[92:95]\n"
      "s_and_b32 s27, s27, s35\n"
      "v_mfma_f32_16x16x32_bf16 v[88:91], v[248:251], v[84:87], v[88:91]\n"
      "s_cmp_lg_u32 s27, s35\n"
      "v_mfma_f32_16x16x32_bf16 v[92:95], v[252:255], v[84:87], v[92:95]\n"
      "s_cbranch_scc1 G_TW_%=\n"
      "G_TWR_%=:\n"
      "s_nop 4\n"
      "v_mul_f32 v88, v20, v88\n"
      "v_mul_f32 v89, v20, v89\n"
      "v_mul_f32 v90, v20, v90\n"
      "v_mul_f32 v91, v20, v91\n"
      "v_mul_f32 v92, v99, v92\n"
      "v_mul_f32 v93, v99, v93\n"
      "v_mul_f32 v94, v99, v94\n"
      "v_mul_f32 v95, v99, v95\n"
      "v_cvt_pk_bf16_f32 v104, v88, v89\n"
      "v_cvt_pk_bf16_f32 v105, v90, v91\n"
      "ds_write_b64 v23, v[104:105]\n"
      "v_cvt_pk_bf16_f32 v106, v92, v93\n"
      "v_cvt_pk_bf16_f32 v107, v94, v95\n"
      "ds_write_b64 v23, v[106:107] offset:4608\n"
      "ds_add_u32 v1, v26 offset:48\n"
      "s_cmp_eq_u32 s31, 7\n"
      "s_cbranch_scc1 G_KV_%=\n"
      "G_KVR_%=:\n"
      "s_add_u32 s31, s31, 1\n"
      "s_and_b32 s31, s31, 7\n"
      "s_cmp_lg_u32 s26, 8\n"
      "s_cbranch_scc1 G_TILE_%=\n"
      "G_DONE_%=:\n"
      "s_setprio 2\n"
      "s_mov_b32 s26, 6\n"
      "s_mov_b32 s63, 8\n"
      "s_cmp_eq_u32 s17, 3\n"
      "s_cbranch_scc1 T_ROLE_%=\n"
      "s_cmp_eq_u32 s17, 0\n"
      "s_cbranch_scc1 L_NEXT_%=\n"
      "s_sub_u32 s85, 5, s17\n"
      "s_mov_b32 s86, 8\n"
      "s_branch O_ROLE_%=\n"
      "G3_PRE_%=:\n"
      "s_and_b32 s28, s16, 1\n"
      "s_mul_i32 s32, s28, 663552\n"
      "s_add_u32 s32, s32, 4194304\n"
      "s_mul_i32 s33, s28, 55296\n"
      "s_add_u32 s33, s33, 36864\n"
      "s_and_b32 s28, s18, 3\n"
      "s_add_u32 s28, s28, 32\n"
      "s_mul_i32 s34, s28, 18432\n"
      "s_add_u32 s34, s34, s32\n"
      "s_mul_i32 s28, s18, 36864\n"
      "s_add_u32 s32, s32, s28\n"
      "s_cmp_lt_u32 s16, 2\n"
      "s_cselect_b32 s36, 3, 1\n"
      "s_lshr_b32 s28, s18, 2\n"
      "s_sub_u32 s28, s28, s22\n"
      "s_and_b32 s28, s28, 3\n"
      "s_lshl_b32 s37, 0x11, s28\n"
      "G3_TILE_%=:\n"
      "ds_read_b32 v96, v2 offset:44\n"
      "ds_read_b64 v[98:99], v24\n"
      "ds_read_b128 v[64:67], v18 offset:0\n"
      "ds_read_b128 v[68:71], v18 offset:1024\n"
      "ds_read_b128 v[72:75], v18 offset:2048\n"
      "ds_read_b128 v[76:79], v18 offset:3072\n"
      "ds_read_b128 v[80:83], v18 offset:4096\n"
      "s_lshl_b32 s28, s31, s84\n"
      "v_add_u32 v23, s28, v3\n"
      "s_lshl_b32 s28, s31, 2\n"
      "s_lshl_b32 s29, 1, s28\n"
      "v_mov_b32 v26, s29\n"
      "s_waitcnt lgkmcnt(5)\n"
      "v_readfirstlane_b32 s27, v96\n"
      "v_mul_f32 v88, v98, v6\n"
      "v_mul_f32 v89, v98, v7\n"
      "v_mul_f32 v90, v98, v8\n"
      "v_mul_f32 v91, v98, v9\n"
      "s_bitcmp1_b32 s27, s26\n"
      "s_cbranch_scc0 G3_XW_%=\n"
      "s_waitcnt lgkmcnt(3)\n"
      "v_mfma_f32_16x16x32_bf16 v[88:91], v[112:115], v[64:67], v[88:91]\n"
      "ds_read_b128 v[84:87], v18 offset:5120\n"
      "v_mul_f32 v20, v99, v5\n"
      "v_mfma_f32_16x16x32_bf16 v[88:91], v[120:123], v[68:71], v[88:91]\n"
      "ds_read_b128 v[64:67], v18 offset:6144\n"
      "s_waitcnt lgkmcnt(3)\n"
      "v_mfma_f32_16x16x32_bf16 v[88:91], v[128:131], v[72:75], v[88:91]\n"
      "ds_read_b128 v[68:71], v18 offset:7168\n"
      "v_mfma_f32_16x16x32_bf16 v[88:91], v[136:139], v[76:79], v[88:91]\n"
      "ds_read_b128 v[72:75], v18 offset:8192\n"
      "ds_add_u32 v1, v25 offset:32\n"
      "s_waitcnt lgkmcnt(4)\n"
      "v_mfma_f32_16x16x32_bf16 v[88:91], v[144:147], v[80:83], v[88:91]\n"
      "ds_read_b128 v[76:79], v18 offset:9216\n"
      "v_mfma_f32_16x16x32_bf16 v[88:91], v[152:155], v[84:87], v[88:91]\n"
      "ds_read_b128 v[80:83], v18 offset:10240\n"
      "s_waitcnt lgkmcnt(4)\n"
      "v_mfma_f32_16x16x32_bf16 v[88:91], v[160:163], v[64:67], v[88:91]\n"
      "ds_read_b128 v[84:87], v18 offset:11264\n"
      "v_mfma_f32_16x16x32_bf16 v[88:91], v[168:171], v[68:71], v[88:91]\n"
      "ds_read_b128 v[64:67], v18 offset:12288\n"
      "s_waitcnt lgkmcnt(3)\n"
      "v_mfma_f32_16x16x32_bf16 v[88:91], v[176:179], v[72:75], v[88:91]\n"
      "ds_read_b128 v[68:71], v18 offset:13312\n"
      "v_mfma_f32_16x16x32_bf16 v[88:91], v[184:187], v[76:79], v[88:91]\n"
      "ds_read_b128 v[72:75], v18 offset:14336\n"
      "s_waitcnt lgkmcnt(3)\n"
      "v_mfma_f32_16x16x32_bf16 v[88:91], v[192:195], v[80:83], v[88:91]\n"
      "ds_read_b128 v[76:79], v18 offset:15360\n"
      "ds_read_b32 v97, v2 offset:20\n"
      "v_mfma_f32_16x16x32_bf16 v[88:91], v[200:203], v[84:87], v[88:91]\n"
      "ds_read_b128 v[80:83], v18 offset:16384\n"
      "s_waitcnt lgkmcnt(4)\n"
      "v_mfma_f32_16x16x32_bf16 v[88:91], v[208:211], v[64:67], v[88:91]\n"
      "ds_read_b128 v[84:87], v18 offset:17408\n"
      "ds_add_u32 v1, v25 offset:36\n"
      "v_mfma_f32_16x16x32_bf16 v[88:91], v[216:219], v[68:71], v[88:91]\n"
      "v_add_u32 v18, s30, v18\n"
      "s_sub_u32 s30, 0, s30\n"
      "s_waitcnt lgkmcnt(3)\n"
      "v_mfma_f32_16x16x32_bf16 v[88:91], v[224:227], v[72:75], v[88:91]\n"
      "v_lshlrev_b32 v25, 4, v25\n"
      "v_add_u32 v24, 128, v24\n"
      "v_mfma_f32_16x16x32_bf16 v[88:91], v[232:235], v[76:79], v[88:91]\n"
      "v_readfirstlane_b32 s27, v97\n"
      "s_lshl_b32 s35, 3, s31\n"
      "s_waitcnt lgkmcnt(1)\n"
      "v_mfma_f32_16x16x32_bf16 v[88:91], v[240:243], v[80:83], v[88:91]\n"
      "s_add_u32 s26, s26, 1\n"
      "s_and_b32 s27, s27, s35\n"
      "v_mfma_f32_16x16x32_bf16 v[88:91], v[248:251], v[84:87], v[88:91]\n"
      "s_cmp_lg_u32 s27, s35\n"
      "s_cbranch_scc1 G3_TW_%=\n"
      "G3_TWR_%=:\n"
      "s_nop 5\n"
      "v_mul_f32 v88, v20, v88\n"
      "v_mul_f32 v89, v20, v89\n"
      "v_mul_f32 v90, v20, v90\n"
      "v_mul_f32 v91, v20, v91\n"
      "v_cvt_pk_bf16_f32 v104, v88, v89\n"
      "v_cvt_pk_bf16_f32 v105, v90, v91\n"
      "s_mov_b32 exec_hi, 0xffff\n"
      "ds_write_b64 v23, v[104:105]\n"
      "s_mov_b32 exec_hi, -1\n"
      "ds_add_u32 v1, v26 offset:48\n"
      "s_cmp_eq_u32 s31, 7\n"
      "s_cbranch_scc1 G3_KV_%=\n"
      "G3_KVR_%=:\n"
      "s_cmp_lg_u32 s36, 0\n"
      "s_cbranch_scc1 G3_WO_%=\n"
      "G3_WOR_%=:\n"
      "s_add_u32 s31, s31, 1\n"
      "s_and_b32 s31, s31, 7\n"
      "s_cmp_lg_u32 s26, 8\n"
      "s_cbranch_scc1 G3_TILE_%=\n"
      "s_cmp_eq_u32 s36, 0\n"
      "s_cbranch_scc1 G_DONE_%=\n"
      "s_cmp_eq_u32 s36, 4\n"
      "s_cbranch_scc1 G3_E1_%=\n"
      "w1_%=:\n"
      "ds_read_b32 v17, v2 offset:40\n"
      "s_waitcnt lgkmcnt(0)\n"
      "v_readfirstlane_b32 s28, v17\n"
      "s_and_b32 s28, s28, s37\n"
      "s_cmp_eq_u32 s28, s37\n"
      "s_cbranch_scc1 w2_%=\n"
      "s_sleep 1\n"
      "s_sub_u32 s21, s21, 1\n"
      "s_cbranch_scc0 w1_%=\n"
      "s_mov_b32 s21, 0\n"
      "w2_%=:\n"
      "s_mov_b32 s33, 73728\n"
      "s_mov_b32 m0, s33\n"
      "s_nop 0\n"
      "buffer_load_dwordx4 v0, s[8:11], s34 offen offset:0 lds\n"
      "buffer_load_dwordx4 v0, s[8:11], s34 offen offset:1024 lds\n"
      "buffer_load_dwordx4 v0, s[8:11], s34 offen offset:2048 lds\n"
      "buffer_load_dwordx4 v0, s[8:11], s34 offen offset:3072 lds\n"
      "s_add_u32 s34, s34, 4096\n"
      "s_add_u32 s33, s33, 4096\n"
      "s_mov_b32 m0, s33\n"
      "s_nop 0\n"
      "buffer_load_dwordx4 v0, s[8:11], s34 offen offset:0 lds\n"
      "buffer_load_dwordx4 v0, s[8:11], s34 offen offset:1024 lds\n"
      "buffer_load_dwordx4 v0, s[8:11], s34 offen offset:2048 lds\n"
      "buffer_load_dwordx4 v0, s[8:11], s34 offen offset:3072 lds\n"
      "s_add_u32 s34, s34, 4096\n"
      "s_add_u32 s33, s33, 4096\n"
      "s_mov_b32 m0, s33\n"
      "s_nop 0\n"
      "buffer_load_dwordx4 v0, s[8:11], s34 offen offset:0 lds\n"
      "buffer_load_dwordx4 v0, s[8:11], s34 offen offset:1024 lds\n"
      "buffer_load_dwordx4 v0, s[8:11], s34 offen offset:2048 lds\n"
      "buffer_load_dwordx4 v0, s[8:11], s34 offen offset:3072 lds\n"
      "s_add_u32 s34, s34, 4096\n"
      "s_add_u32 s33, s33, 4096\n"
      "s_mov_b32 m0, s33\n"
      "s_nop 0\n"
      "buffer_load_dwordx4 v0, s[8:11], s34 offen offset:0 lds\n"
      "buffer_load_dwordx4 v0, s[8:11], s34 offen offset:1024 lds\n"
      "buffer_load_dwordx4 v0, s[8:11], s34 offen offset:2048 lds\n"
      "buffer_load_dwordx4 v0, s[8:11], s34 offen offset:3072 lds\n"
      "s_add_u32 s34, s34, 4096\n"
      "s_add_u32 s33, s33, 4096\n"
      "s_mov_b32 m0, s33\n"
      "s_nop 0\n"
      "buffer_load_dwordx4 v0, s[8:11], s34 offen offset:0 lds\n"
      "buffer_load_dwordx4 v0, s[8:11], s34 offen offset:1024 lds\n"
      "G3_E1_%=:\n"
      "s_waitcnt vmcnt(0)\n"
      "v_mov_b32 v17, 2\n"
      "ds_add_u32 v1, v17 offset:56\n"
      "s_branch G_DONE_%=\n"
      "G3_WO_%=:\n"
      "s_cmp_eq_u32 s36, 4\n"
      "s_cbranch_scc1 G3_WO4_%=\n"
      "s_cmp_eq_u32 s36, 1\n"
      "s_cbranch_scc1 G3_WO1_%=\n"
      "s_mov_b32 m0, s33\n"
      "s_nop 0\n"
      "buffer_load_dwordx4 v0, s[8:11], s32 offen offset:0 lds\n"
      "buffer_load_dwordx4 v0, s[8:11], s32 offen offset:1024 lds\n"
      "buffer_load_dwordx4 v0, s[8:11], s32 offen offset:2048 lds\n"
      "buffer_load_dwordx4 v0, s[8:11], s32 offen offset:3072 lds\n"
      "s_add_u32 s32, s32, 4096\n"
      "s_add_u32 s33, s33, 4096\n"
      "s_mov_b32 m0, s33\n"
      "s_nop 0\n"
      "buffer_load_dwordx4 v0, s[8:11], s32 offen offset:0 lds\n"
      "buffer_load_dwordx4 v0, s[8:11], s32 offen offset:1024 lds\n"
      "buffer_load_dwordx4 v0, s[8:11], s32 offen offset:2048 lds\n"
      "buffer_load_dwordx4 v0, s[8:11], s32 offen offset:3072 lds\n"
      "s_add_u32 s32, s32, 4096\n"
      "s_add_u32 s33, s33, 4096\n"
      "s_mov_b32 m0, s33\n"
      "s_nop 0\n"
      "buffer_load_dwordx4 v0, s[8:11], s32 offen offset:0 lds\n"
      "buffer_load_dwordx4 v0, s[8:11], s32 offen offset:1024 lds\n"
      "buffer_load_dwordx4 v0, s[8:11], s32 offen offset:2048 lds\n"
      "buffer_load_dwordx4 v0, s[8:11], s32 offen offset:3072 lds\n"
      "s_add_u32 s32, s32, 4096\n"
      "s_add_u32 s33, s33, 4096\n"
      "s_mov_b32 m0, s33\n"
      "s_nop 0\n"
      "buffer_load_dwordx4 v0, s[8:11], s32 offen offset:0 lds\n"
      "buffer_load_dwordx4 v0, s[8:11], s32 offen offset:1024 lds\n"
      "buffer_load_dwordx4 v0, s[8:11], s32 offen offset:2048 lds\n"
      "buffer_load_dwordx4 v0, s[8:11], s32 offen offset:3072 lds\n"
      "s_add_u32 s32, s32, 4096\n"
      "s_add_u32 s33, s33, 4096\n"
      "s_mov_b32 m0, s33\n"
      "s_nop 0\n"
      "buffer_load_dwordx4 v0, s[8:11], s32 offen offset:0 lds\n"
      "buffer_load_dwordx4 v0, s[8:11], s32 offen offset:1024 lds\n"
      "s_add_u32 s32, s32, 2048\n"
      "s_add_u32 s33, s33, 2048\n"
      "s_sub_u32 s36, s36, 1\n"
      "s_branch G3_WOR_%=\n"
      "G3_WO1_%=:\n"
      "ds_read_b32 v17, v2 offset:40\n"
      "s_cmp_gt_u32 s16, 1\n"
      "s_cbranch_scc1 G3_WO2_%=\n"
      "s_cmp_lg_u32 s26, 3\n"
      "s_cbranch_scc1 G3_WO2_%=\n"
      "s_waitcnt vmcnt(0)\n"
      "v_mov_b32 v27, 1\n"
      "ds_add_u32 v1, v27 offset:56\n"
      "G3_WO2_%=:\n"
      "s_waitcnt lgkmcnt(0)\n"
      "v_readfirstlane_b32 s27, v17\n"
      "s_and_b32 s27, s27, s37\n"
      "s_cmp_lg_u32 s27, s37\n"
      "s_cbranch_scc1 G3_WOR_%=\n"
      "s_mov_b32 s33, 73728\n"
      "s_mov_b32 m0, s33\n"
      "s_nop 0\n"
      "buffer_load_dwordx4 v0, s[8:11], s34 offen offset:0 lds\n"
      "buffer_load_dwordx4 v0, s[8:11], s34 offen offset:1024 lds\n"
      "buffer_load_dwordx4 v0, s[8:11], s34 offen offset:2048 lds\n"
      "buffer_load_dwordx4 v0, s[8:11], s34 offen offset:3072 lds\n"
      "s_add_u32 s34, s34, 4096\n"
      "s_add_u32 s33, s33, 4096\n"
      "s_mov_b32 m0, s33\n"
      "s_nop 0\n"
      "buffer_load_dwordx4 v0, s[8:11], s34 offen offset:0 lds\n"
      "buffer_load_dwordx4 v0, s[8:11], s34 offen offset:1024 lds\n"
      "buffer_load_dwordx4 v0, s[8:11], s34 offen offset:2048 lds\n"
      "buffer_load_dwordx4 v0, s[8:11], s34 offen offset:3072 lds\n"
      "s_add_u32 s34, s34, 4096\n"
      "s_add_u32 s33, s33, 4096\n"
      "s_mov_b32 m0, s33\n"
      "s_nop 0\n"
      "buffer_load_dwordx4 v0, s[8:11], s34 offen offset:0 lds\n"
      "buffer_load_dwordx4 v0, s[8:11], s34 offen offset:1024 lds\n"
      "buffer_load_dwordx4 v0, s[8:11], s34 offen offset:2048 lds\n"
      "buffer_load_dwordx4 v0, s[8:11], s34 offen offset:3072 lds\n"
      "s_add_u32 s34, s34, 4096\n"
      "s_add_u32 s33, s33, 4096\n"
      "s_mov_b32 m0, s33\n"
      "s_nop 0\n"
      "buffer_load_dwordx4 v0, s[8:11], s34 offen offset:0 lds\n"
      "buffer_load_dwordx4 v0, s[8:11], s34 offen offset:1024 lds\n"
      "buffer_load_dwordx4 v0, s[8:11], s34 offen offset:2048 lds\n"
      "buffer_load_dwordx4 v0, s[8:11], s34 offen offset:3072 lds\n"
      "s_add_u32 s34, s34, 4096\n"
      "s_add_u32 s33, s33, 4096\n"
      "s_mov_b32 m0, s33\n"
      "s_nop 0\n"
      "buffer_load_dwordx4 v0, s[8:11], s34 offen offset:0 lds\n"
      "buffer_load_dwordx4 v0, s[8:11], s34 offen offset:1024 lds\n"
      "s_mov_b32 s36, 4\n"
      "s_branch G3_WOR_%=\n"
      "G3_WO4_%=:\n"
      "s_waitcnt vmcnt(0)\n"
      "v_mov_b32 v17, 2\n"
      "ds_add_u32 v1, v17 offset:56\n"
      "s_mov_b32 s36, 0\n"
      "s_branch G3_WOR_%=\n"
      "G_KV_%=:\n"
      "s_lshl_b32 s28, s18, 12\n"
      "s_add_u32 s28, s28, s20\n"
      "s_lshl_b32 s27, s17, 10\n"
      "s_add_u32 s28, s28, s27\n"
      "s_add_u32 s28, s28, 311296\n"
      "v_min_u32 v104, -2, v104\n"
      "v_min_u32 v105, -2, v105\n"
      "v_min_u32 v106, -2, v106\n"
      "v_min_u32 v107, -2, v107\n"
      "buffer_store_dwordx2 v[104:105], v14, s[8:11], s28 offen sc1\n"
      "buffer_store_dwordx2 v[106:107], v14, s[8:11], s28 offen offset:512 sc1\n"
      "s_branch G_KVR_%=\n"
      "G_XW_%=:\n"
      "s_cmp_eq_u32 s21, 0\n"
      "s_cbranch_scc1 G_DONE_%=\n"
      "s_lshl_b32 s27, 1, s26\n"
      "w3_%=:\n"
      "ds_read_b32 v17, v2 offset:44\n"
      "s_waitcnt lgkmcnt(0)\n"
      "v_readfirstlane_b32 s28, v17\n"
      "s_and_b32 s28, s28, s27\n"
      "s_cmp_eq_u32 s28, s27\n"
      "s_cbranch_scc1 w4_%=\n"
      "s_sleep 1\n"
      "s_sub_u32 s21, s21, 1\n"
      "s_cbranch_scc0 w3_%=\n"
      "s_mov_b32 s21, 0\n"
      "w4_%=:\n"
      "s_branch G_TILE_%=\n"
      "G_TW_%=:\n"
      "w5_%=:\n"
      "ds_read_b32 v17, v2 offset:20\n"
      "s_waitcnt lgkmcnt(0)\n"
      "v_readfirstlane_b32 s28, v17\n"
      "s_and_b32 s28, s28, s35\n"
      "s_cmp_eq_u32 s28, s35\n"
      "s_cbranch_scc1 w6_%=\n"
      "s_sleep 1\n"
      "s_sub_u32 s21, s21, 1\n"
      "s_cbranch_scc0 w5_%=\n"
      "s_mov_b32 s21, 0\n"
      "w6_%=:\n"
      "s_branch G_TWR_%=\n"
      "G3_KV_%=:\n"
      "s_lshl_b32 s28, s18, 12\n"
      "s_add_u32 s28, s28, s20\n"
      "s_lshl_b32 s27, s17, 10\n"
      "s_add_u32 s28, s28, s27\n"
      "s_add_u32 s28, s28, 311296\n"
      "v_min_u32 v104, -2, v104\n"
      "v_min_u32 v105, -2, v105\n"
      "v_min_u32 v106, -2, v106\n"
      "v_min_u32 v107, -2, v107\n"
      "buffer_store_dwordx2 v[104:105], v14, s[8:11], s28 offen sc1\n"
      "buffer_store_dwordx2 v[106:107], v14, s[8:11], s28 offen offset:512 sc1\n"
      "s_branch G3_KVR_%=\n"
      "G3_XW_%=:\n"
      "s_cmp_eq_u32 s21, 0\n"
      "s_cbranch_scc1 G_DONE_%=\n"
      "s_lshl_b32 s27, 1, s26\n"
      "w7_%=:\n"
      "ds_read_b32 v17, v2 offset:44\n"
      "s_waitcnt lgkmcnt(0)\n"
      "v_readfirstlane_b32 s28, v17\n"
      "s_and_b32 s28, s28, s27\n"
      "s_cmp_eq_u32 s28, s27\n"
      "s_cbranch_scc1 w8_%=\n"
      "s_sleep 1\n"
      "s_sub_u32 s21, s21, 1\n"
      "s_cbranch_scc0 w7_%=\n"
      "s_mov_b32 s21, 0\n"
      "w8_%=:\n"
      "s_branch G3_TILE_%=\n"
      "G3_TW_%=:\n"
      "w9_%=:\n"
      "ds_read_b32 v17, v2 offset:20\n"
      "s_waitcnt lgkmcnt(0)\n"
      "v_readfirstlane_b32 s28, v17\n"
      "s_and_b32 s28, s28, s35\n"
      "s_cmp_eq_u32 s28, s35\n"
      "s_cbranch_scc1 w10_%=\n"
      "s_sleep 1\n"
      "s_sub_u32 s21, s21, 1\n"
      "s_cbranch_scc0 w9_%=\n"
      "s_mov_b32 s21, 0\n"
      "w10_%=:\n"
      "s_branch G3_TWR_%=\n"
      "HELP_%=:\n"
      "s_setprio 2\n"
      "s_mov_b32 s26, 0\n"
      "s_mov_b32 s63, 6\n"
      "s_cmp_eq_u32 s17, 3\n"
      "s_cbranch_scc1 T_ROLE_%=\n"
      "s_sub_u32 s85, s17, 1\n"
      "s_mov_b32 s86, 3\n"
      "s_cmp_lg_u32 s17, 0\n"
      "s_cbranch_scc1 O_ROLE_%=\n"
      "s_cmp_eq_u32 s16, 0\n"
      "s_cselect_b32 s26, 0, 1\n"
      "s_mov_b32 s89, 8\n"
      "S_ROLE_%=:\n"
      "v_lshrrev_b32 v18, 8, v0\n"
      "v_lshlrev_b32 v18, 9, v18\n"
      "v_add_u32 v18, v18, v4\n"
      "v_subrev_u32 v18, 160128, v18\n"
      "s_and_b32 s28, s16, 1\n"
      "s_lshl_b32 s28, s28, 3\n"
      "s_add_u32 s28, s28, s26\n"
      "s_lshl_b32 s28, s28, 7\n"
      "v_add_u32 v19, s28, v4\n"
      "v_mov_b32 v32, 1.0\n"
      "v_mov_b32 v33, 0x3727c5ac\n"
      "s_mov_b32 s44, 0xbae38e39\n"
      "s_mov_b32 s45, 0x3ae38e39\n"
      "s_add_u32 s37, s20, 147456\n"
      "s_add_u32 s47, s22, s26\n"
      "s_and_b32 s47, s47, 7\n"
      "s_mul_i32 s47, s47, 20480\n"
      "s_add_u32 s47, s47, s37\n"
      "S_OPW_%=:\n"
      "s_lshl_b32 s27, 1, s26\n"
      "w11_%=:\n"
      "ds_read_b32 v17, v2 offset:40\n"
      "s_waitcnt lgkmcnt(0)\n"
      "v_readfirstlane_b32 s28, v17\n"
      "s_and_b32 s28, s28, s27\n"
      "s_cmp_eq_u32 s28, s27\n"
      "s_cbranch_scc1 w12_%=\n"
      "s_sleep 1\n"
      "s_sub_u32 s21, s21, 1\n"
      "s_cbranch_scc0 w11_%=\n"
      "s_mov_b32 s21, 0\n"
      "w12_%=:\n"
      "S_POLL_%=:\n"
      "s_add_u32 s38, s47, 18432\n"
      "buffer_load_dwordx2 v[24:25], v18, s[8:11], s38 offen offset:0 sc1\n"
      "buffer_load_dwordx2 v[26:27], v18, s[8:11], s38 offen offset:128 sc1\n"
      "buffer_load_dwordx2 v[28:29], v18, s[8:11], s38 offen offset:256 sc1\n"
      "buffer_load_dwordx2 v[30:31], v18, s[8:11], s38 offen offset:384 sc1\n"
      "s_waitcnt vmcnt(0)\n"
      "v_max3_u32 v17, v24, v25, v26\n"
      "v_max3_u32 v17, v17, v27, v28\n"
      "v_max3_u32 v17, v17, v29, v30\n"
      "v_max_u32 v17, v17, v31\n"
      "v_cmp_eq_u32 vcc, -1, v17\n"
      "s_cbranch_vccz S_GOT_%=\n"
      "s_sleep 4\n"
      "s_sub_u32 s21, s21, 16\n"
      "s_cbranch_scc0 S_POLL_%=\n"
      "s_mov_b32 s21, 0\n"
      "S_GOT_%=:\n"
      "s_mov_b32 s54, 1\n"
      "S_LOOP_%=:\n"
      "ds_read_b32 v20, v2 offset:40\n"
      "s_and_b32 s46, s26, 1\n"
      "s_mul_i32 s46, s46, 18432\n"
      "s_add_u32 s53, s26, 6\n"
      "s_and_b32 s53, s53, 7\n"
      "s_lshl_b32 s53, s53, 2\n"
      "s_lshl_b32 s53, 4, s53\n"
      "s_cmp_lt_u32 s26, 2\n"
      "s_cselect_b32 s28, 0, 32\n"
      "v_add_u32 v34, s28, v2\n"
      "s_lshl_b32 s27, 1, s26\n"
      "v_mov_b32 v35, s27\n"
      "w13_%=:\n"
      "ds_read_b32 v17, v34 offset:0\n"
      "s_waitcnt lgkmcnt(0)\n"
      "v_readfirstlane_b32 s28, v17\n"
      "s_and_b32 s28, s28, s53\n"
      "s_cmp_eq_u32 s28, s53\n"
      "s_cbranch_scc1 w14_%=\n"
      "s_sleep 1\n"
      "s_sub_u32 s21, s21, 1\n"
      "s_cbranch_scc0 w13_%=\n"
      "s_mov_b32 s21, 0\n"
      "w14_%=:\n"
      "s_mov_b32 m0, s46\n"
      "s_nop 0\n"
      "buffer_load_dwordx4 v0, s[8:11], s47 offen offset:0 sc1 lds\n"
      "buffer_load_dwordx4 v0, s[8:11], s47 offen offset:1024 sc1 lds\n"
      "buffer_load_dwordx4 v0, s[8:11], s47 offen offset:2048 sc1 lds\n"
      "buffer_load_dwordx4 v0, s[8:11], s47 offen offset:3072 sc1 lds\n"
      "s_add_u32 s47, s47, 4096\n"
      "s_add_u32 s46, s46, 4096\n"
      "s_mov_b32 m0, s46\n"
      "s_nop 0\n"
      "buffer_load_dwordx4 v0, s[8:11], s47 offen offset:0 sc1 lds\n"
      "buffer_load_dwordx4 v0, s[8:11], s47 offen offset:1024 sc1 lds\n"
      "buffer_load_dwordx4 v0, s[8:11], s47 offen offset:2048 sc1 lds\n"
      "buffer_load_dwordx4 v0, s[8:11], s47 offen offset:3072 sc1 lds\n"
      "s_add_u32 s47, s47, 4096\n"
      "s_add_u32 s46, s46, 4096\n"
      "s_mov_b32 m0, s46\n"
      "s_nop 0\n"
      "buffer_load_dwordx4 v0, s[8:11], s47 offen offset:0 sc1 lds\n"
      "s_add_u32 s47, s47, 1024\n"
      "s_add_u32 s46, s46, 1024\n"
      "w15_%=:\n"
      "ds_read_b32 v17, v34 offset:4\n"
      "s_waitcnt lgkmcnt(0)\n"
      "v_readfirstlane_b32 s28, v17\n"
      "s_and_b32 s28, s28, s53\n"
      "s_cmp_eq_u32 s28, s53\n"
      "s_cbranch_scc1 w16_%=\n"
      "s_sleep 1\n"
      "s_sub_u32 s21, s21, 1\n"
      "s_cbranch_scc0 w15_%=\n"
      "s_mov_b32 s21, 0\n"
      "w16_%=:\n"
      "s_mov_b32 m0, s46\n"
      "s_nop 0\n"
      "buffer_load_dwordx4 v0, s[8:11], s47 offen offset:0 sc1 lds\n"
      "buffer_load_dwordx4 v0, s[8:11], s47 offen offset:1024 sc1 lds\n"
      "buffer_load_dwordx4 v0, s[8:11], s47 offen offset:2048 sc1 lds\n"
      "buffer_load_dwordx4 v0, s[8:11], s47 offen offset:3072 sc1 lds\n"
      "s_add_u32 s47, s47, 4096\n"
      "s_add_u32 s46, s46, 4096\n"
      "s_mov_b32 m0, s46\n"
      "s_nop 0\n"
      "buffer_load_dwordx4 v0, s[8:11], s47 offen offset:0 sc1 lds\n"
      "buffer_load_dwordx4 v0, s[8:11], s47 offen offset:1024 sc1 lds\n"
      "buffer_load_dwordx4 v0, s[8:11], s47 offen offset:2048 sc1 lds\n"
      "buffer_load_dwordx4 v0, s[8:11], s47 offen offset:3072 sc1 lds\n"
      "s_add_u32 s47, s47, 4096\n"
      "s_add_u32 s46, s46, 4096\n"
      "s_mov_b32 m0, s46\n"
      "s_nop 0\n"
      "buffer_load_dwordx4 v0, s[8:11], s47 offen offset:0 sc1 lds\n"
      "s_cmp_eq_u32 s54, 0\n"
      "s_cbranch_scc1 S_NP_%=\n"
      "v_add_f32 v24, v24, v26\n"
      "v_add_f32 v25, v25, v27\n"
      "v_add_f32 v28, v28, v30\n"
      "v_add_f32 v29, v29, v31\n"
      "v_add_f32 v24, v24, v28\n"
      "v_add_f32 v25, v25, v29\n"
      "s_nop 0\n"
      "v_mfma_f32_16x16x4_f32 v[36:39], v32, v24, 0\n"
      "v_mfma_f32_16x16x4_f32 v[40:43], v32, v25, 0\n"
      "s_nop 8\n"
      "v_mul_f32 v36, s44, v36\n"
      "v_fma_f32 v40, s45, v40, v33\n"
      "v_fma_f32 v40, -v36, v36, v40\n"
      "v_max_f32 v40, v40, v33\n"
      "v_rsq_f32 v37, v40\n"
      "s_nop 0\n"
      "ds_write_b64 v19, v[36:37]\n"
      "v_add_u32 v19, 128, v19\n"
      "S_NP_%=:\n"
      "v_readfirstlane_b32 s52, v20\n"
      "s_add_u32 s26, s26, 1\n"
      "s_cmp_eq_u32 s26, s89\n"
      "s_cbranch_scc1 S_END_%=\n"
      "s_add_u32 s47, s22, s26\n"
      "s_and_b32 s47, s47, 7\n"
      "s_mul_i32 s47, s47, 20480\n"
      "s_add_u32 s47, s47, s37\n"
      "s_bitcmp1_b32 s52, s26\n"
      "s_cbranch_scc0 S_LATE_%=\n"
      "s_add_u32 s38, s47, 18432\n"
      "buffer_load_dwordx2 v[24:25], v18, s[8:11], s38 offen offset:0 sc1\n"
      "buffer_load_dwordx2 v[26:27], v18, s[8:11], s38 offen offset:128 sc1\n"
      "buffer_load_dwordx2 v[28:29], v18, s[8:11], s38 offen offset:256 sc1\n"
      "buffer_load_dwordx2 v[30:31], v18, s[8:11], s38 offen offset:384 sc1\n"
      "s_waitcnt vmcnt(4)\n"
      "ds_add_u32 v1, v35 offset:44\n"
      "s_waitcnt vmcnt(0)\n"
      "v_max3_u32 v17, v24, v25, v26\n"
      "v_max3_u32 v17, v17, v27, v28\n"
      "v_max3_u32 v17, v17, v29, v30\n"
      "v_max_u32 v17, v17, v31\n"
      "v_cmp_eq_u32 vcc, -1, v17\n"
      "s_cmp_eq_u32 s16, 0\n"
      "s_cbranch_scc1 S_W_%=\n"
      "S_WR_%=:\n"
      "s_cbranch_vccz S_GOT_%=\n"
      "s_branch S_POLL_%=\n"
      "S_LATE_%=:\n"
      "s_waitcnt vmcnt(0)\n"
      "ds_add_u32 v1, v35 offset:44\n"
      "s_branch S_OPW_%=\n"
      "S_W_%=:\n"
      "s_cmp_lg_u32 s26, 2\n"
      "s_cbranch_scc1 sw17_%=\n"
      "s_add_u32 s28, s24, 0\n"
      "s_add_u32 s29, s24, 18432\n"
      "buffer_load_dwordx4 v[112:115], v0, s[8:11], s28 offen offset:0\n"
      "buffer_load_dwordx4 v[116:119], v0, s[8:11], s29 offen offset:0\n"
      "buffer_load_dwordx4 v[120:123], v0, s[8:11], s28 offen offset:1024\n"
      "buffer_load_dwordx4 v[124:127], v0, s[8:11], s29 offen offset:1024\n"
      "buffer_load_dwordx4 v[128:131], v0, s[8:11], s28 offen offset:2048\n"
      "buffer_load_dwordx4 v[132:135], v0, s[8:11], s29 offen offset:2048\n"
      "s_branch S_WR_%=\n"
      "sw17_%=:\n"
      "s_cmp_lg_u32 s26, 3\n"
      "s_cbranch_scc1 sw18_%=\n"
      "s_add_u32 s28, s24, 0\n"
      "s_add_u32 s29, s24, 18432\n"
      "buffer_load_dwordx4 v[136:139], v0, s[8:11], s28 offen offset:3072\n"
      "buffer_load_dwordx4 v[140:143], v0, s[8:11], s29 offen offset:3072\n"
      "s_add_u32 s28, s24, 4096\n"
      "s_add_u32 s29, s24, 22528\n"
      "buffer_load_dwordx4 v[144:147], v0, s[8:11], s28 offen offset:0\n"
      "buffer_load_dwordx4 v[148:151], v0, s[8:11], s29 offen offset:0\n"
      "buffer_load_dwordx4 v[152:155], v0, s[8:11], s28 offen offset:1024\n"
      "buffer_load_dwordx4 v[156:159], v0, s[8:11], s29 offen offset:1024\n"
      "s_branch S_WR_%=\n"
      "sw18_%=:\n"
      "s_cmp_lg_u32 s26, 4\n"
      "s_cbranch_scc1 sw19_%=\n"
      "s_add_u32 s28, s24, 4096\n"
      "s_add_u32 s29, s24, 22528\n"
      "buffer_load_dwordx4 v[160:163], v0, s[8:11], s28 offen offset:2048\n"
      "buffer_load_dwordx4 v[164:167], v0, s[8:11], s29 offen offset:2048\n"
      "buffer_load_dwordx4 v[168:171], v0, s[8:11], s28 offen offset:3072\n"
      "buffer_load_dwordx4 v[172:175], v0, s[8:11], s29 offen offset:3072\n"
      "s_add_u32 s28, s24, 8192\n"
      "s_add_u32 s29, s24, 26624\n"
      "buffer_load_dwordx4 v[176:179], v0, s[8:11], s28 offen offset:0\n"
      "buffer_load_dwordx4 v[180:183], v0, s[8:11], s29 offen offset:0\n"
      "s_branch S_WR_%=\n"
      "sw19_%=:\n"
      "s_cmp_lg_u32 s26, 5\n"
      "s_cbranch_scc1 sw20_%=\n"
      "s_add_u32 s28, s24, 8192\n"
      "s_add_u32 s29, s24, 26624\n"
      "buffer_load_dwordx4 v[184:187], v0, s[8:11], s28 offen offset:1024\n"
      "buffer_load_dwordx4 v[188:191], v0, s[8:11], s29 offen offset:1024\n"
      "buffer_load_dwordx4 v[192:195], v0, s[8:11], s28 offen offset:2048\n"
      "buffer_load_dwordx4 v[196:199], v0, s[8:11], s29 offen offset:2048\n"
      "buffer_load_dwordx4 v[200:203], v0, s[8:11], s28 offen offset:3072\n"
      "buffer_load_dwordx4 v[204:207], v0, s[8:11], s29 offen offset:3072\n"
      "s_branch S_WR_%=\n"
      "sw20_%=:\n"
      "s_cmp_lg_u32 s26, 6\n"
      "s_cbranch_scc1 sw21_%=\n"
      "s_add_u32 s28, s24, 12288\n"
      "s_add_u32 s29, s24, 30720\n"
      "buffer_load_dwordx4 v[208:211], v0, s[8:11], s28 offen offset:0\n"
      "buffer_load_dwordx4 v[212:215], v0, s[8:11], s29 offen offset:0\n"
      "buffer_load_dwordx4 v[216:219], v0, s[8:11], s28 offen offset:1024\n"
      "buffer_load_dwordx4 v[220:223], v0, s[8:11], s29 offen offset:1024\n"
      "buffer_load_dwordx4 v[224:227], v0, s[8:11], s28 offen offset:2048\n"
      "buffer_load_dwordx4 v[228:231], v0, s[8:11], s29 offen offset:2048\n"
      "s_branch S_WR_%=\n"
      "sw21_%=:\n"
      "s_cmp_lg_u32 s26, 7\n"
      "s_cbranch_scc1 sw22_%=\n"
      "s_add_u32 s28, s24, 12288\n"
      "s_add_u32 s29, s24, 30720\n"
      "buffer_load_dwordx4 v[232:235], v0, s[8:11], s28 offen offset:3072\n"
      "buffer_load_dwordx4 v[236:239], v0, s[8:11], s29 offen offset:3072\n"
      "s_add_u32 s28, s24, 16384\n"
      "s_add_u32 s29, s24, 34816\n"
      "buffer_load_dwordx4 v[240:243], v0, s[8:11], s28 offen offset:0\n"
      "buffer_load_dwordx4 v[244:247], v0, s[8:11], s29 offen offset:0\n"
      "buffer_load_dwordx4 v[248:251], v0, s[8:11], s28 offen offset:1024\n"
      "buffer_load_dwordx4 v[252:255], v0, s[8:11], s29 offen offset:1024\n"
      "s_branch S_WR_%=\n"
      "sw22_%=:\n"
      "s_branch S_WR_%=\n"
      "S_END_%=:\n"
      "s_waitcnt vmcnt(0)\n"
      "ds_add_u32 v1, v35 offset:44\n"
      "s_cmp_eq_u32 s89, 1\n"
      "s_cbranch_scc1 G_ROLE_%=\n"
      "s_branch L_NEXT_%=\n"
      "T_ROLE_%=:\n"
      "s_cmp_eq_u32 s16, 0\n"
      "s_cselect_b32 s56, 0x43201765, s56\n"
      "s_cselect_b32 s62, 3, s62\n"
      "s_cmp_eq_u32 s16, 1\n"
      "s_cselect_b32 s56, 0x54320176, s56\n"
      "s_cselect_b32 s62, 2, s62\n"
      "s_cmp_eq_u32 s16, 2\n"
      "s_cselect_b32 s56, 0x65432017, s56\n"
      "s_cselect_b32 s62, 1, s62\n"
      "s_cmp_eq_u32 s16, 3\n"
      "s_cselect_b32 s56, 0x76543201, s56\n"
      "s_cselect_b32 s62, 0, s62\n"
      "s_lshl_b32 s28, s26, 2\n"
      "s_lshr_b32 s56, s56, s28\n"
      "s_lshl_b32 s58, s18, 10\n"
      "s_add_u32 s58, s58, s20\n"
      "s_lshl_b32 s59, s18, 7\n"
      "s_add_u32 s59, s59, s20\n"
      "s_add_u32 s59, s59, 16384\n"
      "s_mov_b32 s60, 0x3f803f80\n"
      "s_mov_b32 s64, 0\n"
      "T_C2_%=:\n"
      "v_lshrrev_b32 v30, 4, v0\n"
      "v_and_b32 v31, 15, v30\n"
      "v_lshrrev_b32 v32, 4, v30\n"
      "v_and_b32 v33, 7, v30\n"
      "v_min_u32 v34, 2, v32\n"
      "v_mul_u32_u24 v23, 1152, v34\n"
      "v_mul_u32_u24 v34, 1152, v32\n"
      "v_lshlrev_b32 v35, 3, v31\n"
      "v_add_u32 v18, v34, v35\n"
      "v_add_u32 v18, 133696, v18\n"
      "v_add_u32 v23, v23, v35\n"
      "v_add_u32 v23, 156736, v23\n"
      "v_lshlrev_b32 v35, 3, v33\n"
      "v_mov_b32 v36, 138368\n"
      "v_mov_b32 v37, 129152\n"
      "v_cndmask_b32 v36, v36, v37, s[70:71]\n"
      "v_add3_u32 v19, v36, v34, v35\n"
      "v_mov_b32 v36, 156804\n"
      "v_mov_b32 v37, 156800\n"
      "v_cndmask_b32 v36, v36, v37, s[70:71]\n"
      "v_add_u32 v20, v36, v35\n"
      "v_lshlrev_b32 v21, 3, v30\n"
      "v_add_u32 v21, 147712, v21\n"
      "v_lshrrev_b32 v22, 2, v30\n"
      "v_lshlrev_b32 v22, 3, v22\n"
      "v_add_u32 v22, 159040, v22\n"
      "v_lshlrev_b32 v35, 4, v33\n"
      "v_mov_b32 v38, 0x80000000\n"
      "v_lshl_add_u32 v24, v32, 8, v35\n"
      "v_add_u32 v36, 512, v24\n"
      "v_cndmask_b32 v24, v36, v24, s[70:71]\n"
      "v_cmp_gt_u32 vcc, 32, v30\n"
      "s_nop 1\n"
      "v_cndmask_b32 v24, v38, v24, vcc\n"
      "v_lshlrev_b32 v35, 3, v33\n"
      "v_add_u32 v36, 4, v35\n"
      "v_cndmask_b32 v25, v36, v35, s[70:71]\n"
      "v_cmp_gt_u32 vcc, 16, v30\n"
      "s_nop 1\n"
      "v_cndmask_b32 v25, v38, v25, vcc\n"
      "v_lshlrev_b32 v36, 2, v32\n"
      "v_sub_u32 v36, v36, v33\n"
      "v_mov_b32 v38, 0xf149f2ca\n"
      "v_add_u32 v37, -1, v36\n"
      "v_cmp_gt_u32 vcc, 8, v37\n"
      "s_nop 1\n"
      "v_cndmask_b32 v26, v38, 0, vcc\n"
      "v_add_u32 v37, 0, v36\n"
      "v_cmp_gt_u32 vcc, 8, v37\n"
      "s_nop 1\n"
      "v_cndmask_b32 v27, v38, 0, vcc\n"
      "v_add_u32 v37, 1, v36\n"
      "v_cmp_gt_u32 vcc, 8, v37\n"
      "s_nop 1\n"
      "v_cndmask_b32 v28, v38, 0, vcc\n"
      "v_add_u32 v37, 2, v36\n"
      "v_cmp_gt_u32 vcc, 8, v37\n"
      "s_nop 1\n"
      "v_cndmask_b32 v29, v38, 0, vcc\n"
      "s_mov_b32 s61, 0\n"
      "T_LOOP_%=:\n"
      "s_and_b32 s44, s56, 15\n"
      "s_sub_u32 s28, s44, s61\n"
      "s_mov_b32 s61, s44\n"
      "s_lshl_b32 s45, s28, 7\n"
      "s_lshl_b32 s46, s28, 9\n"
      "v_add_u32 v18, s45, v18\n"
      "v_add_u32 v19, s45, v19\n"
      "v_add_u32 v20, s45, v20\n"
      "v_add_u32 v23, s45, v23\n"
      "v_add_u32 v22, s45, v22\n"
      "v_add_u32 v21, s46, v21\n"
      "s_lshl_b32 s27, s44, 2\n"
      "s_mov_b32 s28, 0x44\n"
      "s_mov_b32 s29, 0\n"
      "s_lshl_b64 s[28:29], s[28:29], s27\n"
      "s_lshr_b64 s[28:29], s[28:29], 4\n"
      "s_mov_b32 s48, s28\n"
      "s_mul_i32 s47, s44, 18432\n"
      "s_cmp_eq_u32 s44, 0\n"
      "s_cbranch_scc1 T_HALO_%=\n"
      "T_RD_%=:\n"
      "ds_read_b32 v17, v2 offset:48\n"
      "ds_read_b64 v[30:31], v18 offset:0\n"
      "ds_read_b64 v[32:33], v18 offset:9216\n"
      "ds_read_b64 v[34:35], v19 offset:0\n"
      "ds_read_b64 v[38:39], v23 offset:0\n"
      "ds_read_b32 v40, v20 offset:0\n"
      "ds_read_b64 v[88:89], v18 offset:64\n"
      "ds_read_b64 v[90:91], v18 offset:9280\n"
      "ds_read_b64 v[92:93], v19 offset:64\n"
      "ds_read_b64 v[96:97], v23 offset:64\n"
      "ds_read_b32 v98, v20 offset:64\n"
      "s_add_u32 s49, s58, s47\n"
      "s_add_u32 s50, s59, s47\n"
      "s_waitcnt lgkmcnt(5)\n"
      "v_readfirstlane_b32 s27, v17\n"
      "v_cndmask_b32 v36, v34, 0, s[70:71]\n"
      "v_cndmask_b32 v37, v35, 0, s[70:71]\n"
      "v_cndmask_b32 v34, 0, v34, s[70:71]\n"
      "v_cndmask_b32 v35, 0, v35, s[70:71]\n"
      "s_and_b32 s27, s27, s48\n"
      "s_cmp_lg_u32 s27, s48\n"
      "s_cbranch_scc1 T_QW_%=\n"
      "s_waitcnt lgkmcnt(0)\n"
      "v_cndmask_b32 v94, v92, 0, s[70:71]\n"
      "v_cndmask_b32 v95, v93, 0, s[70:71]\n"
      "v_cndmask_b32 v92, 0, v92, s[70:71]\n"
      "v_cndmask_b32 v93, 0, v93, s[70:71]\n"
      "v_cndmask_b32 v42, 0, v40, s[72:73]\n"
      "v_cndmask_b32 v100, 0, v98, s[72:73]\n"
      "v_cndmask_b32 v43, 0, v40, s[74:75]\n"
      "v_cndmask_b32 v101, 0, v98, s[74:75]\n"
      "v_mov_b32 v44, 0\n"
      "v_mov_b32 v102, 0\n"
      "v_mov_b32 v45, 0\n"
      "v_mov_b32 v103, 0\n"
      "v_mov_b32 v41, 0\n"
      "v_mov_b32 v99, 0\n"
      "v_mfma_f32_16x16x32_bf16 v[30:33], v[30:33], v[34:37], v[26:29]\n"
      "v_mfma_f32_16x16x32_bf16 v[88:91], v[88:91], v[92:95], v[26:29]\n"
      "ds_read_b64_tr_b16 v[46:47], v21 offset:0\n"
      "ds_read_b64_tr_b16 v[104:105], v21 offset:256\n"
      "ds_read_b64_tr_b16 v[48:49], v21 offset:4608\n"
      "ds_read_b64_tr_b16 v[106:107], v21 offset:4864\n"
      "v_mfma_f32_16x16x32_bf16 v[30:33], v[38:41], v[42:45], v[30:33]\n"
      "v_mfma_f32_16x16x32_bf16 v[88:91], v[96:99], v[100:103], v[88:91]\n"
      "ds_read_b64_tr_b16 v[50:51], v22 offset:0\n"
      "ds_read_b64_tr_b16 v[108:109], v22 offset:64\n"
      "v_mov_b32 v62, s60\n"
      "s_lshr_b32 s56, s56, 4\n"
      "s_cmp_eq_u32 s26, s62\n"
      "s_cbranch_scc1 T_KVQ_%=\n"
      "T_KVR_%=:\n"
      "s_cmp_eq_u32 s44, 0\n"
      "s_cbranch_scc1 T_H0_%=\n"
      "T_H0R_%=:\n"
      "s_add_u32 s26, s26, 1\n"
      "v_max3_f32 v34, v30, v31, v32\n"
      "v_max3_f32 v92, v88, v89, v90\n"
      "v_max_f32 v34, v34, v33\n"
      "v_max_f32 v92, v92, v91\n"
      "v_mov_b32 v35, v34\n"
      "v_mov_b32 v93, v92\n"
      "s_nop 0\n"
      "v_permlane16_swap_b32 v34, v35\n"
      "v_permlane16_swap_b32 v92, v93\n"
      "v_max_f32 v34, v34, v35\n"
      "v_max_f32 v92, v92, v93\n"
      "v_mov_b32 v35, v34\n"
      "v_mov_b32 v93, v92\n"
      "s_nop 0\n"
      "v_permlane32_swap_b32 v34, v35\n"
      "v_permlane32_swap_b32 v92, v93\n"
      "v_max_f32 v34, v34, v35\n"
      "v_max_f32 v92, v92, v93\n"
      "v_sub_f32 v30, v30, v34\n"
      "v_sub_f32 v88, v88, v92\n"
      "v_sub_f32 v31, v31, v34\n"
      "v_sub_f32 v89, v89, v92\n"
      "v_sub_f32 v32, v32, v34\n"
      "v_sub_f32 v90, v90, v92\n"
      "v_sub_f32 v33, v33, v34\n"
      "v_sub_f32 v91, v91, v92\n"
      "v_exp_f32 v30, v30\n"
      "v_exp_f32 v88, v88\n"
      "v_exp_f32 v31, v31\n"
      "v_exp_f32 v89, v89\n"
      "v_exp_f32 v32, v32\n"
      "v_exp_f32 v90, v90\n"
      "v_exp_f32 v33, v33\n"
      "v_exp_f32 v91, v91\n"
      "s_waitcnt lgkmcnt(0)\n"
      "v_cndmask_b32 v42, v62, v50, s[76:77]\n"
      "v_cndmask_b32 v100, v62, v108, s[76:77]\n"
      "v_cndmask_b32 v43, v62, v51, s[76:77]\n"
      "v_cndmask_b32 v101, v62, v109, s[76:77]\n"
      "v_cndmask_b32 v44, v62, v50, s[78:79]\n"
      "v_cndmask_b32 v102, v62, v108, s[78:79]\n"
      "v_cndmask_b32 v45, v62, v51, s[78:79]\n"
      "v_cndmask_b32 v103, v62, v109, s[78:79]\n"
      "v_cvt_pk_bf16_f32 v34, v30, v31\n"
      "v_cvt_pk_bf16_f32 v92, v88, v89\n"
      "v_cvt_pk_bf16_f32 v35, v32, v33\n"
      "v_cvt_pk_bf16_f32 v93, v90, v91\n"
      "v_cndmask_b32 v36, v34, 0, s[70:71]\n"
      "v_cndmask_b32 v94, v92, 0, s[70:71]\n"
      "v_cndmask_b32 v37, v35, 0, s[70:71]\n"
      "v_cndmask_b32 v95, v93, 0, s[70:71]\n"
      "v_cndmask_b32 v34, 0, v34, s[70:71]\n"
      "v_cndmask_b32 v92, 0, v92, s[70:71]\n"
      "v_cndmask_b32 v35, 0, v35, s[70:71]\n"
      "v_cndmask_b32 v93, 0, v93, s[70:71]\n"
      "s_nop 0\n"
      "v_mfma_f32_16x16x32_bf16 v[38:41], v[42:45], v[34:37], 0\n"
      "v_mfma_f32_16x16x32_bf16 v[96:99], v[100:103], v[92:95], 0\n"
      "v_mfma_f32_16x16x32_bf16 v[30:33], v[46:49], v[34:37], 0\n"
      "v_mfma_f32_16x16x32_bf16 v[88:91], v[104:107], v[92:95], 0\n"
      "s_nop 4\n"
      "v_cndmask_b32 v50, v38, v40, s[70:71]\n"
      "v_cndmask_b32 v108, v96, v98, s[70:71]\n"
      "v_rcp_f32 v50, v50\n"
      "v_rcp_f32 v108, v108\n"
      "v_cndmask_b32 v34, v40, v38, s[70:71]\n"
      "v_cndmask_b32 v92, v98, v96, s[70:71]\n"
      "v_cndmask_b32 v35, v41, v39, s[70:71]\n"
      "v_cndmask_b32 v93, v99, v97, s[70:71]\n"
      "v_mul_f32 v30, v30, v50\n"
      "v_mul_f32 v88, v88, v108\n"
      "v_mul_f32 v31, v31, v50\n"
      "v_mul_f32 v89, v89, v108\n"
      "v_mul_f32 v32, v32, v50\n"
      "v_mul_f32 v90, v90, v108\n"
      "v_mul_f32 v33, v33, v50\n"
      "v_mul_f32 v91, v91, v108\n"
      "v_mul_f32 v34, v34, v50\n"
      "v_mul_f32 v92, v92, v108\n"
      "v_mul_f32 v35, v35, v50\n"
      "v_mul_f32 v93, v93, v108\n"
      "v_cvt_pk_bf16_f32 v46, v30, v31\n"
      "v_cvt_pk_bf16_f32 v104, v88, v89\n"
      "v_cvt_pk_bf16_f32 v47, v32, v33\n"
      "v_cvt_pk_bf16_f32 v105, v90, v91\n"
      "v_cvt_pk_bf16_f32 v34, v34, v35\n"
      "v_cvt_pk_bf16_f32 v92, v92, v93\n"
      "s_cmp_eq_u32 s64, 0\n"
      "s_cbranch_scc1 tf23_%=\n"
      "v_mov_b32 v55, s64\n"
      "s_waitcnt vmcnt(0)\n"
      "buffer_store_dword v53, v25, s[8:11], s51 offen sc1\n"
      "buffer_store_dword v54, v25, s[8:11], s51 offen offset:64 sc1\n"
      "ds_add_u32 v1, v55 offset:52\n"
      "s_mov_b32 s64, 0\n"
      "tf23_%=:\n"
      "v_min_u32 v53, -2, v34\n"
      "v_min_u32 v54, -2, v92\n"
      "v_mov_b32 v30, v46\n"
      "v_mov_b32 v88, v104\n"
      "v_mov_b32 v31, v47\n"
      "v_mov_b32 v89, v105\n"
      "v_permlane32_swap_b32 v30, v48\n"
      "v_permlane32_swap_b32 v88, v106\n"
      "v_permlane32_swap_b32 v31, v49\n"
      "v_permlane32_swap_b32 v89, v107\n"
      "buffer_store_dwordx4 v[46:49], v24, s[8:11], s49 offen offset:0 sc1\n"
      "buffer_store_dwordx4 v[104:107], v24, s[8:11], s49 offen offset:128 sc1\n"
      "s_lshl_b32 s64, 1, s44\n"
      "s_mov_b32 s51, s50\n"
      "s_cmp_eq_u32 s16, 0\n"
      "s_cbranch_scc1 T_W_%=\n"
      "T_WR_%=:\n"
      "s_cmp_lg_u32 s26, s63\n"
      "s_cbranch_scc1 T_LOOP_%=\n"
      "T_END_%=:\n"
      "s_cmp_eq_u32 s64, 0\n"
      "s_cbranch_scc1 tf24_%=\n"
      "v_mov_b32 v55, s64\n"
      "s_waitcnt vmcnt(0)\n"
      "buffer_store_dword v53, v25, s[8:11], s51 offen sc1\n"
      "buffer_store_dword v54, v25, s[8:11], s51 offen offset:64 sc1\n"
      "ds_add_u32 v1, v55 offset:52\n"
      "s_mov_b32 s64, 0\n"
      "tf24_%=:\n"
      "s_branch L_NEXT_%=\n"
      "T_QW_%=:\n"
      "s_cmp_eq_u32 s21, 0\n"
      "s_cbranch_scc1 T_END_%=\n"
      "s_cmp_eq_u32 s64, 0\n"
      "s_cbranch_scc1 tf25_%=\n"
      "v_mov_b32 v55, s64\n"
      "s_waitcnt vmcnt(0)\n"
      "buffer_store_dword v53, v25, s[8:11], s51 offen sc1\n"
      "buffer_store_dword v54, v25, s[8:11], s51 offen offset:64 sc1\n"
      "ds_add_u32 v1, v55 offset:52\n"
      "s_mov_b32 s64, 0\n"
      "tf25_%=:\n"
      "w26_%=:\n"
      "ds_read_b32 v17, v2 offset:48\n"
      "s_waitcnt lgkmcnt(0)\n"
      "v_readfirstlane_b32 s28, v17\n"
      "s_and_b32 s28, s28, s48\n"
      "s_cmp_eq_u32 s28, s48\n"
      "s_cbranch_scc1 w27_%=\n"
      "s_sleep 1\n"
      "s_sub_u32 s21, s21, 1\n"
      "s_cbranch_scc0 w26_%=\n"
      "s_mov_b32 s21, 0\n"
      "w27_%=:\n"
      "s_branch T_RD_%=\n"
      "T_W_%=:\n"
      "T_W1_%=:\n"
      "s_cmp_lg_u32 s26, 1\n"
      "s_cbranch_scc1 tw28_%=\n"
      "s_add_u32 s28, s24, 0\n"
      "buffer_load_dwordx4 v[112:115], v0, s[8:11], s28 offen offset:0\n"
      "buffer_load_dwordx4 v[120:123], v0, s[8:11], s28 offen offset:1024\n"
      "buffer_load_dwordx4 v[128:131], v0, s[8:11], s28 offen offset:2048\n"
      "buffer_load_dwordx4 v[136:139], v0, s[8:11], s28 offen offset:3072\n"
      "s_add_u32 s28, s24, 4096\n"
      "buffer_load_dwordx4 v[144:147], v0, s[8:11], s28 offen offset:0\n"
      "buffer_load_dwordx4 v[152:155], v0, s[8:11], s28 offen offset:1024\n"
      "s_branch T_WR_%=\n"
      "tw28_%=:\n"
      "s_cmp_lg_u32 s26, 2\n"
      "s_cbranch_scc1 tw29_%=\n"
      "s_add_u32 s28, s24, 4096\n"
      "buffer_load_dwordx4 v[160:163], v0, s[8:11], s28 offen offset:2048\n"
      "buffer_load_dwordx4 v[168:171], v0, s[8:11], s28 offen offset:3072\n"
      "s_add_u32 s28, s24, 8192\n"
      "buffer_load_dwordx4 v[176:179], v0, s[8:11], s28 offen offset:0\n"
      "buffer_load_dwordx4 v[184:187], v0, s[8:11], s28 offen offset:1024\n"
      "buffer_load_dwordx4 v[192:195], v0, s[8:11], s28 offen offset:2048\n"
      "buffer_load_dwordx4 v[200:203], v0, s[8:11], s28 offen offset:3072\n"
      "s_branch T_WR_%=\n"
      "tw29_%=:\n"
      "s_cmp_lg_u32 s26, 3\n"
      "s_cbranch_scc1 tw30_%=\n"
      "s_add_u32 s28, s24, 12288\n"
      "buffer_load_dwordx4 v[208:211], v0, s[8:11], s28 offen offset:0\n"
      "buffer_load_dwordx4 v[216:219], v0, s[8:11], s28 offen offset:1024\n"
      "buffer_load_dwordx4 v[224:227], v0, s[8:11], s28 offen offset:2048\n"
      "buffer_load_dwordx4 v[232:235], v0, s[8:11], s28 offen offset:3072\n"
      "s_add_u32 s28, s24, 16384\n"
      "buffer_load_dwordx4 v[240:243], v0, s[8:11], s28 offen offset:0\n"
      "buffer_load_dwordx4 v[248:251], v0, s[8:11], s28 offen offset:1024\n"
      "s_branch T_WR_%=\n"
      "tw30_%=:\n"
      "s_branch T_WR_%=\n"
      "T_KVQ_%=:\n"
      "s_and_b32 s28, s19, 7\n"
      "s_cbranch_scc0 T_KVR_%=\n"
      "s_lshl_b32 s28, s18, 12\n"
      "s_add_u32 s28, s28, s20\n"
      "s_add_u32 s28, s28, 0xffedc000\n"
      "v_and_b32 v52, 511, v0\n"
      "buffer_load_dwordx4 v[64:67], v0, s[8:11], s28 offen offset:0 sc1\n"
      "buffer_load_dwordx4 v[68:71], v0, s[8:11], s28 offen offset:1024 sc1\n"
      "buffer_load_dwordx4 v[72:75], v0, s[8:11], s28 offen offset:2048 sc1\n"
      "buffer_load_dwordx4 v[76:79], v52, s[8:11], s28 offen offset:3072 sc1\n"
      "s_branch T_KVR_%=\n"
      "T_HALO_%=:\n"
      "s_and_b32 s28, s19, 7\n"
      "s_cbranch_scc0 T_RD_%=\n"
      "s_cmp_eq_u32 s64, 0\n"
      "s_cbranch_scc1 tf31_%=\n"
      "v_mov_b32 v55, s64\n"
      "s_waitcnt vmcnt(0)\n"
      "buffer_store_dword v53, v25, s[8:11], s51 offen sc1\n"
      "buffer_store_dword v54, v25, s[8:11], s51 offen offset:64 sc1\n"
      "ds_add_u32 v1, v55 offset:52\n"
      "s_mov_b32 s64, 0\n"
      "tf31_%=:\n"
      "s_waitcnt vmcnt(2)\n"
      "s_branch T_HC_%=\n"
      "T_HP_%=:\n"
      "s_lshl_b32 s28, s18, 12\n"
      "s_add_u32 s28, s28, s20\n"
      "s_add_u32 s28, s28, 0xffedc000\n"
      "v_and_b32 v52, 511, v0\n"
      "buffer_load_dwordx4 v[64:67], v0, s[8:11], s28 offen offset:0 sc1\n"
      "buffer_load_dwordx4 v[68:71], v0, s[8:11], s28 offen offset:1024 sc1\n"
      "buffer_load_dwordx4 v[72:75], v0, s[8:11], s28 offen offset:2048 sc1\n"
      "buffer_load_dwordx4 v[76:79], v52, s[8:11], s28 offen offset:3072 sc1\n"
      "s_waitcnt vmcnt(0)\n"
      "T_HC_%=:\n"
      "v_and_b32 v52, 511, v0\n"
      "v_max3_u32 v36, v64, v65, v66\n"
      "v_max3_u32 v36, v36, v67, v68\n"
      "v_max3_u32 v36, v36, v69, v70\n"
      "v_max3_u32 v36, v36, v71, v72\n"
      "v_max3_u32 v36, v36, v73, v74\n"
      "v_max3_u32 v36, v36, v75, v76\n"
      "v_max3_u32 v36, v36, v77, v78\n"
      "v_max_u32 v36, v36, v79\n"
      "v_cmp_eq_u32 vcc, -1, v36\n"
      "s_cbranch_vccz T_HQ_%=\n"
      "s_sleep 4\n"
      "s_sub_u32 s21, s21, 16\n"
      "s_cbranch_scc0 T_HP_%=\n"
      "s_mov_b32 s21, 0\n"
      "T_HQ_%=:\n"
      "v_lshrrev_b32 v36, 7, v52\n"
      "v_mul_u32_u24 v36, 1152, v36\n"
      "v_and_b32 v37, 127, v52\n"
      "v_add_u32 v36, v36, v37\n"
      "v_add_u32 v36, 129024, v36\n"
      "v_lshrrev_b32 v37, 9, v0\n"
      "v_mul_u32_u24 v37, 4608, v37\n"
      "v_add_u32 v38, v36, v37\n"
      "v_add_u32 v39, v52, v37\n"
      "v_add_u32 v39, 147456, v39\n"
      "ds_write_b128 v38, v[64:67]\n"
      "ds_write_b128 v38, v[68:71] offset:9216\n"
      "ds_write_b128 v39, v[72:75]\n"
      "s_mov_b64 exec, 0xffffff\n"
      "ds_write_b128 v36, v[76:79] offset:27648\n"
      "s_mov_b64 exec, -1\n"
      "s_branch T_RD_%=\n"
      "T_H0_%=:\n"
      "s_and_b32 s28, s19, 7\n"
      "s_cbranch_scc1 T_H0R_%=\n"
      "s_nop 7\n"
      "s_mov_b32 exec_hi, 0\n"
      "v_mov_b32 v30, 0xf149f2ca\n"
      "v_mov_b32 v31, 0xf149f2ca\n"
      "v_mov_b32 v32, 0xf149f2ca\n"
      "v_mov_b32 v33, 0xf149f2ca\n"
      "s_mov_b32 exec_hi, -1\n"
      "s_branch T_H0R_%=\n"
      "O_ROLE_%=:\n"
      "v_and_b32 v55, 0x1ff, v0\n"
      "v_lshrrev_b32 v54, 9, v0\n"
      "v_lshl_add_u32 v55, v54, 3, v55\n"
      "v_and_b32 v54, 0xf0, v0\n"
      "v_lshrrev_b32 v54, 1, v54\n"
      "v_and_or_b32 v54, v0, s65, v54\n"
      "s_and_b32 s28, s16, 1\n"
      "s_mul_i32 s28, s28, 55296\n"
      "v_add_u32 v62, s28, v15\n"
      "s_cmp_lg_u32 s16, 0\n"
      "s_cbranch_scc1 O_TASK_%=\n"
      "s_cmp_gt_u32 s85, 1\n"
      "s_cbranch_scc1 O_TASK_%=\n"
      "w32_%=:\n"
      "ds_read_b32 v17, v2 offset:36\n"
      "s_waitcnt lgkmcnt(0)\n"
      "v_readfirstlane_b32 s28, v17\n"
      "s_and_b32 s28, s28, 4\n"
      "s_cmp_eq_u32 s28, 4\n"
      "s_cbranch_scc1 w33_%=\n"
      "s_sleep 1\n"
      "s_sub_u32 s21, s21, 1\n"
      "s_cbranch_scc0 w32_%=\n"
      "s_mov_b32 s21, 0\n"
      "w33_%=:\n"
      "s_add_u32 s28, s24, 0\n"
      "s_add_u32 s29, s24, 18432\n"
      "buffer_load_dwordx4 v[112:115], v0, s[8:11], s28 offen offset:0\n"
      "buffer_load_dwordx4 v[116:119], v0, s[8:11], s29 offen offset:0\n"
      "buffer_load_dwordx4 v[120:123], v0, s[8:11], s28 offen offset:1024\n"
      "buffer_load_dwordx4 v[124:127], v0, s[8:11], s29 offen offset:1024\n"
      "buffer_load_dwordx4 v[128:131], v0, s[8:11], s28 offen offset:2048\n"
      "buffer_load_dwordx4 v[132:135], v0, s[8:11], s29 offen offset:2048\n"
      "buffer_load_dwordx4 v[136:139], v0, s[8:11], s28 offen offset:3072\n"
      "buffer_load_dwordx4 v[140:143], v0, s[8:11], s29 offen offset:3072\n"
      "s_add_u32 s28, s24, 4096\n"
      "s_add_u32 s29, s24, 22528\n"
      "buffer_load_dwordx4 v[144:147], v0, s[8:11], s28 offen offset:0\n"
      "buffer_load_dwordx4 v[148:151], v0, s[8:11], s29 offen offset:0\n"
      "buffer_load_dwordx4 v[152:155], v0, s[8:11], s28 offen offset:1024\n"
      "buffer_load_dwordx4 v[156:159], v0, s[8:11], s29 offen offset:1024\n"
      "buffer_load_dwordx4 v[160:163], v0, s[8:11], s28 offen offset:2048\n"
      "buffer_load_dwordx4 v[164:167], v0, s[8:11], s29 offen offset:2048\n"
      "buffer_load_dwordx4 v[168:171], v0, s[8:11], s28 offen offset:3072\n"
      "buffer_load_dwordx4 v[172:175], v0, s[8:11], s29 offen offset:3072\n"
      "s_add_u32 s28, s24, 8192\n"
      "s_add_u32 s29, s24, 26624\n"
      "buffer_load_dwordx4 v[176:179], v0, s[8:11], s28 offen offset:0\n"
      "buffer_load_dwordx4 v[180:183], v0, s[8:11], s29 offen offset:0\n"
      "buffer_load_dwordx4 v[184:187], v0, s[8:11], s28 offen offset:1024\n"
      "buffer_load_dwordx4 v[188:191], v0, s[8:11], s29 offen offset:1024\n"
      "buffer_load_dwordx4 v[192:195], v0, s[8:11], s28 offen offset:2048\n"
      "buffer_load_dwordx4 v[196:199], v0, s[8:11], s29 offen offset:2048\n"
      "buffer_load_dwordx4 v[200:203], v0, s[8:11], s28 offen offset:3072\n"
      "buffer_load_dwordx4 v[204:207], v0, s[8:11], s29 offen offset:3072\n"
      "s_add_u32 s28, s24, 12288\n"
      "s_add_u32 s29, s24, 30720\n"
      "buffer_load_dwordx4 v[208:211], v0, s[8:11], s28 offen offset:0\n"
      "buffer_load_dwordx4 v[212:215], v0, s[8:11], s29 offen offset:0\n"
      "buffer_load_dwordx4 v[216:219], v0, s[8:11], s28 offen offset:1024\n"
      "buffer_load_dwordx4 v[220:223], v0, s[8:11], s29 offen offset:1024\n"
      "buffer_load_dwordx4 v[224:227], v0, s[8:11], s28 offen offset:2048\n"
      "buffer_load_dwordx4 v[228:231], v0, s[8:11], s29 offen offset:2048\n"
      "buffer_load_dwordx4 v[232:235], v0, s[8:11], s28 offen offset:3072\n"
      "buffer_load_dwordx4 v[236:239], v0, s[8:11], s29 offen offset:3072\n"
      "s_add_u32 s28, s24, 16384\n"
      "s_add_u32 s29, s24, 34816\n"
      "buffer_load_dwordx4 v[240:243], v0, s[8:11], s28 offen offset:0\n"
      "buffer_load_dwordx4 v[244:247], v0, s[8:11], s29 offen offset:0\n"
      "buffer_load_dwordx4 v[248:251], v0, s[8:11], s28 offen offset:1024\n"
      "buffer_load_dwordx4 v[252:255], v0, s[8:11], s29 offen offset:1024\n"
      "O_TASK_%=:\n"
      "s_add_u32 s40, s22, s85\n"
      "s_add_u32 s40, s40, 1\n"
      "s_and_b32 s40, s40, 7\n"
      "s_mul_i32 s41, s40, 18432\n"
      "s_add_u32 s41, s41, s20\n"
      "s_mul_i32 s42, s40, 20480\n"
      "s_add_u32 s42, s42, s20\n"
      "s_add_u32 s42, s42, 147456\n"
      "s_and_b32 s43, s18, 3\n"
      "s_lshl_b32 s43, s43, 9\n"
      "s_add_u32 s43, s43, 16384\n"
      "s_and_b32 s28, s40, 3\n"
      "s_lshr_b32 s29, s18, 2\n"
      "s_cmp_eq_u32 s28, s29\n"
      "s_cselect_b32 s88, 3, 1\n"
      "s_lshl_b32 s27, 1, s40\n"
      "w34_%=:\n"
      "ds_read_b32 v17, v2 offset:52\n"
      "s_waitcnt lgkmcnt(0)\n"
      "v_readfirstlane_b32 s28, v17\n"
      "s_and_b32 s28, s28, s27\n"
      "s_cmp_eq_u32 s28, s27\n"
      "s_cbranch_scc1 w35_%=\n"
      "s_sleep 1\n"
      "s_sub_u32 s21, s21, 1\n"
      "s_cbranch_scc0 w34_%=\n"
      "s_mov_b32 s21, 0\n"
      "w35_%=:\n"
      "O_HP_%=:\n"
      "s_add_u32 s38, s41, 16384\n"
      "buffer_load_dwordx2 v[80:81], v54, s[8:11], s38 offen offset:0 sc1\n"
      "buffer_load_dwordx2 v[82:83], v54, s[8:11], s38 offen offset:128 sc1\n"
      "buffer_load_dwordx2 v[84:85], v54, s[8:11], s38 offen offset:1024 sc1\n"
      "buffer_load_dwordx2 v[86:87], v54, s[8:11], s38 offen offset:1152 sc1\n"
      "s_waitcnt vmcnt(0)\n"
      "v_max3_u32 v17, v80, v81, v82\n"
      "v_max3_u32 v17, v17, v83, v84\n"
      "v_max3_u32 v17, v17, v85, v86\n"
      "v_max_u32 v17, v17, v87\n"
      "v_cmp_eq_u32 vcc, -1, v17\n"
      "s_cbranch_vccz O_ISS_%=\n"
      "s_sleep 4\n"
      "s_sub_u32 s21, s21, 16\n"
      "s_cbranch_scc0 O_HP_%=\n"
      "s_mov_b32 s21, 0\n"
      "O_ISS_%=:\n"
      "s_lshl_b32 s28, s18, 10\n"
      "s_add_u32 s28, s28, s42\n"
      "s_add_u32 s29, s42, s43\n"
      "s_add_u32 s39, s41, 12288\n"
      "buffer_load_dwordx4 v[56:59], v0, s[8:11], s28 offen offset:0 sc1\n"
      "buffer_load_dwordx2 v[60:61], v55, s[8:11], s29 offen sc1\n"
      "buffer_load_dwordx4 v[64:67], v0, s[8:11], s39 offen offset:0 sc1\n"
      "buffer_load_dwordx4 v[68:71], v0, s[8:11], s39 offen offset:1024 sc1\n"
      "buffer_load_dwordx4 v[72:75], v0, s[8:11], s39 offen offset:2048 sc1\n"
      "buffer_load_dwordx4 v[76:79], v0, s[8:11], s39 offen offset:3072 sc1\n"
      "s_add_u32 s38, s41, 3072\n"
      "buffer_load_dwordx4 v[88:91], v0, s[8:11], s41 offen offset:0 sc1\n"
      "buffer_load_dwordx4 v[92:95], v0, s[8:11], s41 offen offset:1024 sc1\n"
      "buffer_load_dwordx4 v[96:99], v0, s[8:11], s41 offen offset:2048 sc1\n"
      "buffer_load_dwordx4 v[100:103], v0, s[8:11], s38 offen offset:0 sc1\n"
      "buffer_load_dwordx4 v[104:107], v0, s[8:11], s38 offen offset:1024 sc1\n"
      "buffer_load_dwordx4 v[108:111], v0, s[8:11], s38 offen offset:2048 sc1\n"
      "w36_%=:\n"
      "ds_read_b32 v17, v2 offset:56\n"
      "s_waitcnt lgkmcnt(0)\n"
      "v_readfirstlane_b32 s28, v17\n"
      "s_and_b32 s28, s28, s88\n"
      "s_cmp_eq_u32 s28, s88\n"
      "s_cbranch_scc1 w37_%=\n"
      "s_sleep 1\n"
      "s_sub_u32 s21, s21, 1\n"
      "s_cbranch_scc0 w36_%=\n"
      "s_mov_b32 s21, 0\n"
      "w37_%=:\n"
      "s_add_u32 s38, s41, 6144\n"
      "s_add_u32 s39, s41, 9216\n"
      "s_cmp_eq_u32 s88, 3\n"
      "s_cbranch_scc1 O_X_%=\n"
      "ds_read_b128 v[30:33], v62 offset:12288\n"
      "ds_read_b128 v[34:37], v62 offset:30720\n"
      "ds_read_b128 v[38:41], v62 offset:13312\n"
      "ds_read_b128 v[42:45], v62 offset:31744\n"
      "ds_read_b128 v[46:49], v62 offset:14336\n"
      "ds_read_b128 v[50:53], v62 offset:32768\n"
      "s_waitcnt vmcnt(6)\n"
      "v_lshlrev_b32 v18, 16, v56\n"
      "v_and_b32 v19, 0xffff0000, v56\n"
      "v_lshlrev_b32 v20, 16, v57\n"
      "v_and_b32 v21, 0xffff0000, v57\n"
      "v_lshlrev_b32 v22, 16, v58\n"
      "v_and_b32 v23, 0xffff0000, v58\n"
      "v_lshlrev_b32 v24, 16, v59\n"
      "v_and_b32 v25, 0xffff0000, v59\n"
      "s_waitcnt lgkmcnt(4)\n"
      "v_mfma_f32_16x16x32_bf16 v[18:21], v[30:33], v[64:67], v[18:21]\n"
      "v_mfma_f32_16x16x32_bf16 v[22:25], v[34:37], v[64:67], v[22:25]\n"
      "ds_read_b128 v[30:33], v62 offset:15360\n"
      "ds_read_b128 v[34:37], v62 offset:33792\n"
      "s_waitcnt lgkmcnt(4)\n"
      "v_mfma_f32_16x16x32_bf16 v[18:21], v[38:41], v[68:71], v[18:21]\n"
      "v_mfma_f32_16x16x32_bf16 v[22:25], v[42:45], v[68:71], v[22:25]\n"
      "ds_read_b128 v[38:41], v62 offset:16384\n"
      "ds_read_b128 v[42:45], v62 offset:34816\n"
      "buffer_load_dwordx4 v[64:67], v0, s[8:11], s38 offen offset:0 sc1\n"
      "s_waitcnt lgkmcnt(4)\n"
      "v_mfma_f32_16x16x32_bf16 v[18:21], v[46:49], v[72:75], v[18:21]\n"
      "v_mfma_f32_16x16x32_bf16 v[22:25], v[50:53], v[72:75], v[22:25]\n"
      "ds_read_b128 v[46:49], v62 offset:17408\n"
      "ds_read_b128 v[50:53], v62 offset:35840\n"
      "buffer_load_dwordx4 v[68:71], v0, s[8:11], s38 offen offset:1024 sc1\n"
      "s_waitcnt lgkmcnt(4)\n"
      "v_mfma_f32_16x16x32_bf16 v[18:21], v[30:33], v[76:79], v[18:21]\n"
      "v_mfma_f32_16x16x32_bf16 v[22:25], v[34:37], v[76:79], v[22:25]\n"
      "ds_read_b128 v[30:33], v62 offset:0\n"
      "ds_read_b128 v[34:37], v62 offset:18432\n"
      "buffer_load_dwordx4 v[72:75], v0, s[8:11], s38 offen offset:2048 sc1\n"
      "s_waitcnt lgkmcnt(4)\n"
      "v_mfma_f32_16x16x32_bf16 v[18:21], v[38:41], v[80:83], v[18:21]\n"
      "v_mfma_f32_16x16x32_bf16 v[22:25], v[42:45], v[80:83], v[22:25]\n"
      "ds_read_b128 v[38:41], v62 offset:1024\n"
      "ds_read_b128 v[42:45], v62 offset:19456\n"
      "buffer_load_dwordx4 v[76:79], v0, s[8:11], s39 offen offset:0 sc1\n"
      "s_waitcnt lgkmcnt(4)\n"
      "v_mfma_f32_16x16x32_bf16 v[18:21], v[46:49], v[84:87], v[18:21]\n"
      "v_mfma_f32_16x16x32_bf16 v[22:25], v[50:53], v[84:87], v[22:25]\n"
      "ds_read_b128 v[46:49], v62 offset:2048\n"
      "ds_read_b128 v[50:53], v62 offset:20480\n"
      "buffer_load_dwordx4 v[80:83], v0, s[8:11], s39 offen offset:1024 sc1\n"
      "s_waitcnt vmcnt(5)\n"
      "s_waitcnt lgkmcnt(4)\n"
      "v_mfma_f32_16x16x32_bf16 v[18:21], v[30:33], v[88:91], v[18:21]\n"
      "v_mfma_f32_16x16x32_bf16 v[22:25], v[34:37], v[88:91], v[22:25]\n"
      "ds_read_b128 v[30:33], v62 offset:3072\n"
      "ds_read_b128 v[34:37], v62 offset:21504\n"
      "buffer_load_dwordx4 v[84:87], v0, s[8:11], s39 offen offset:2048 sc1\n"
      "s_waitcnt lgkmcnt(4)\n"
      "v_mfma_f32_16x16x32_bf16 v[18:21], v[38:41], v[92:95], v[18:21]\n"
      "v_mfma_f32_16x16x32_bf16 v[22:25], v[42:45], v[92:95], v[22:25]\n"
      "ds_read_b128 v[38:41], v62 offset:4096\n"
      "ds_read_b128 v[42:45], v62 offset:22528\n"
      "s_waitcnt lgkmcnt(4)\n"
      "v_mfma_f32_16x16x32_bf16 v[18:21], v[46:49], v[96:99], v[18:21]\n"
      "v_mfma_f32_16x16x32_bf16 v[22:25], v[50:53], v[96:99], v[22:25]\n"
      "ds_read_b128 v[46:49], v62 offset:5120\n"
      "ds_read_b128 v[50:53], v62 offset:23552\n"
      "s_waitcnt lgkmcnt(4)\n"
      "v_mfma_f32_16x16x32_bf16 v[18:21], v[30:33], v[100:103], v[18:21]\n"
      "v_mfma_f32_16x16x32_bf16 v[22:25], v[34:37], v[100:103], v[22:25]\n"
      "ds_read_b128 v[30:33], v62 offset:6144\n"
      "ds_read_b128 v[34:37], v62 offset:24576\n"
      "s_waitcnt lgkmcnt(4)\n"
      "v_mfma_f32_16x16x32_bf16 v[18:21], v[38:41], v[104:107], v[18:21]\n"
      "v_mfma_f32_16x16x32_bf16 v[22:25], v[42:45], v[104:107], v[22:25]\n"
      "ds_read_b128 v[38:41], v62 offset:7168\n"
      "ds_read_b128 v[42:45], v62 offset:25600\n"
      "s_waitcnt lgkmcnt(4)\n"
      "v_mfma_f32_16x16x32_bf16 v[18:21], v[46:49], v[108:111], v[18:21]\n"
      "v_mfma_f32_16x16x32_bf16 v[22:25], v[50:53], v[108:111], v[22:25]\n"
      "ds_read_b128 v[46:49], v62 offset:8192\n"
      "ds_read_b128 v[50:53], v62 offset:26624\n"
      "s_waitcnt vmcnt(0)\n"
      "s_waitcnt lgkmcnt(4)\n"
      "v_mfma_f32_16x16x32_bf16 v[18:21], v[30:33], v[64:67], v[18:21]\n"
      "v_mfma_f32_16x16x32_bf16 v[22:25], v[34:37], v[64:67], v[22:25]\n"
      "ds_read_b128 v[30:33], v62 offset:9216\n"
      "ds_read_b128 v[34:37], v62 offset:27648\n"
      "s_waitcnt lgkmcnt(4)\n"
      "v_mfma_f32_16x16x32_bf16 v[18:21], v[38:41], v[68:71], v[18:21]\n"
      "v_mfma_f32_16x16x32_bf16 v[22:25], v[42:45], v[68:71], v[22:25]\n"
      "ds_read_b128 v[38:41], v62 offset:10240\n"
      "ds_read_b128 v[42:45], v62 offset:28672\n"
      "s_waitcnt lgkmcnt(4)\n"
      "v_mfma_f32_16x16x32_bf16 v[18:21], v[46:49], v[72:75], v[18:21]\n"
      "v_mfma_f32_16x16x32_bf16 v[22:25], v[50:53], v[72:75], v[22:25]\n"
      "ds_read_b128 v[46:49], v62 offset:11264\n"
      "ds_read_b128 v[50:53], v62 offset:29696\n"
      "s_waitcnt lgkmcnt(4)\n"
      "v_mfma_f32_16x16x32_bf16 v[18:21], v[30:33], v[76:79], v[18:21]\n"
      "v_mfma_f32_16x16x32_bf16 v[22:25], v[34:37], v[76:79], v[22:25]\n"
      "s_waitcnt lgkmcnt(2)\n"
      "v_mfma_f32_16x16x32_bf16 v[18:21], v[38:41], v[80:83], v[18:21]\n"
      "v_mfma_f32_16x16x32_bf16 v[22:25], v[42:45], v[80:83], v[22:25]\n"
      "s_waitcnt lgkmcnt(0)\n"
      "v_mfma_f32_16x16x32_bf16 v[18:21], v[46:49], v[84:87], v[18:21]\n"
      "v_mfma_f32_16x16x32_bf16 v[22:25], v[50:53], v[84:87], v[22:25]\n"
      "O_EPI_%=:\n"
      "s_add_u32 s42, s42, 376832\n"
      "s_lshl_b32 s28, s18, 10\n"
      "s_add_u32 s28, s28, s42\n"
      "s_add_u32 s29, s42, s43\n"
      "s_nop 2\n"
      "v_cvt_pk_bf16_f32 v30, v18, v19\n"
      "v_cvt_pk_bf16_f32 v31, v20, v21\n"
      "v_cvt_pk_bf16_f32 v32, v22, v23\n"
      "v_cvt_pk_bf16_f32 v33, v24, v25\n"
      "v_cvt_pk_bf16_f32 v34, v26, v27\n"
      "v_cvt_pk_bf16_f32 v35, v28, v29\n"
      "s_cmp_eq_u32 s16, 3\n"
      "s_cbranch_scc1 O_OUT_%=\n"
      "buffer_store_dwordx4 v[30:33], v0, s[8:11], s28 offen sc1\n"
      "v_add_f32 v36, v18, v19\n"
      "v_mul_f32 v37, v18, v18\n"
      "v_fmac_f32 v37, v19, v19\n"
      "v_add_f32 v36, v36, v20\n"
      "v_fmac_f32 v37, v20, v20\n"
      "v_add_f32 v36, v36, v21\n"
      "v_fmac_f32 v37, v21, v21\n"
      "v_add_f32 v36, v36, v22\n"
      "v_fmac_f32 v37, v22, v22\n"
      "v_add_f32 v36, v36, v23\n"
      "v_fmac_f32 v37, v23, v23\n"
      "v_add_f32 v36, v36, v24\n"
      "v_fmac_f32 v37, v24, v24\n"
      "v_add_f32 v36, v36, v25\n"
      "v_fmac_f32 v37, v25, v25\n"
      "s_cmp_lg_u32 s88, 3\n"
      "s_cbranch_scc1 O_NX_%=\n"
      "buffer_store_dwordx2 v[34:35], v55, s[8:11], s29 offen sc1\n"
      "v_add_f32 v36, v36, v26\n"
      "v_fmac_f32 v37, v26, v26\n"
      "v_add_f32 v36, v36, v27\n"
      "v_fmac_f32 v37, v27, v27\n"
      "v_add_f32 v36, v36, v28\n"
      "v_fmac_f32 v37, v28, v28\n"
      "v_add_f32 v36, v36, v29\n"
      "v_fmac_f32 v37, v29, v29\n"
      "O_NX_%=:\n"
      "v_mov_b32 v38, 1.0\n"
      "s_lshl_b32 s28, s18, 7\n"
      "s_add_u32 s28, s28, s42\n"
      "v_mfma_f32_16x16x4_f32 v[40:43], v38, v36, 0\n"
      "v_mfma_f32_16x16x4_f32 v[44:47], v38, v37, 0\n"
      "s_add_u32 s28, s28, 18432\n"
      "s_nop 7\n"
      "v_min_u32 v40, -2, v40\n"
      "v_min_u32 v41, -2, v44\n"
      "s_waitcnt vmcnt(0)\n"
      "buffer_store_dwordx2 v[40:41], v16, s[8:11], s28 offen sc1\n"
      "O_FIN_%=:\n"
      "s_lshl_b32 s27, 1, s85\n"
      "v_mov_b32 v17, s27\n"
      "ds_add_u32 v1, v17 offset:72\n"
      "s_add_u32 s85, s85, 2\n"
      "s_cmp_lt_u32 s85, s86\n"
      "s_cbranch_scc1 O_TASK_%=\n"
      "s_branch L_NEXT_%=\n"
      "O_X_%=:\n"
      "ds_read_b128 v[30:33], v62 offset:12288\n"
      "ds_read_b128 v[34:37], v62 offset:30720\n"
      "ds_read_b128 v[38:41], v15 offset:49152\n"
      "ds_read_b128 v[42:45], v62 offset:13312\n"
      "ds_read_b128 v[46:49], v62 offset:31744\n"
      "ds_read_b128 v[50:53], v15 offset:50176\n"
      "s_waitcnt vmcnt(6)\n"
      "v_lshlrev_b32 v18, 16, v56\n"
      "v_and_b32 v19, 0xffff0000, v56\n"
      "v_lshlrev_b32 v20, 16, v57\n"
      "v_and_b32 v21, 0xffff0000, v57\n"
      "v_lshlrev_b32 v22, 16, v58\n"
      "v_and_b32 v23, 0xffff0000, v58\n"
      "v_lshlrev_b32 v24, 16, v59\n"
      "v_and_b32 v25, 0xffff0000, v59\n"
      "v_lshlrev_b32 v26, 16, v60\n"
      "v_and_b32 v27, 0xffff0000, v60\n"
      "v_lshlrev_b32 v28, 16, v61\n"
      "v_and_b32 v29, 0xffff0000, v61\n"
      "s_waitcnt lgkmcnt(3)\n"
      "v_mfma_f32_16x16x32_bf16 v[18:21], v[30:33], v[64:67], v[18:21]\n"
      "v_mfma_f32_16x16x32_bf16 v[22:25], v[34:37], v[64:67], v[22:25]\n"
      "v_mfma_f32_16x16x32_bf16 v[26:29], v[38:41], v[64:67], v[26:29]\n"
      "ds_read_b128 v[30:33], v62 offset:14336\n"
      "ds_read_b128 v[34:37], v62 offset:32768\n"
      "ds_read_b128 v[38:41], v15 offset:51200\n"
      "s_waitcnt lgkmcnt(3)\n"
      "v_mfma_f32_16x16x32_bf16 v[18:21], v[42:45], v[68:71], v[18:21]\n"
      "v_mfma_f32_16x16x32_bf16 v[22:25], v[46:49], v[68:71], v[22:25]\n"
      "v_mfma_f32_16x16x32_bf16 v[26:29], v[50:53], v[68:71], v[26:29]\n"
      "ds_read_b128 v[42:45], v62 offset:15360\n"
      "ds_read_b128 v[46:49], v62 offset:33792\n"
      "ds_read_b128 v[50:53], v15 offset:52224\n"
      "buffer_load_dwordx4 v[64:67], v0, s[8:11], s38 offen offset:0 sc1\n"
      "s_waitcnt lgkmcnt(3)\n"
      "v_mfma_f32_16x16x32_bf16 v[18:21], v[30:33], v[72:75], v[18:21]\n"
      "v_mfma_f32_16x16x32_bf16 v[22:25], v[34:37], v[72:75], v[22:25]\n"
      "v_mfma_f32_16x16x32_bf16 v[26:29], v[38:41], v[72:75], v[26:29]\n"
      "ds_read_b128 v[30:33], v62 offset:16384\n"
      "ds_read_b128 v[34:37], v62 offset:34816\n"
      "ds_read_b128 v[38:41], v15 offset:53248\n"
      "buffer_load_dwordx4 v[68:71], v0, s[8:11], s38 offen offset:1024 sc1\n"
      "s_waitcnt lgkmcnt(3)\n"
      "v_mfma_f32_16x16x32_bf16 v[18:21], v[42:45], v[76:79], v[18:21]\n"
      "v_mfma_f32_16x16x32_bf16 v[22:25], v[46:49], v[76:79], v[22:25]\n"
      "v_mfma_f32_16x16x32_bf16 v[26:29], v[50:53], v[76:79], v[26:29]\n"
      "ds_read_b128 v[42:45], v62 offset:17408\n"
      "ds_read_b128 v[46:49], v62 offset:35840\n"
      "ds_read_b128 v[50:53], v15 offset:54272\n"
      "buffer_load_dwordx4 v[72:75], v0, s[8:11], s38 offen offset:2048 sc1\n"
      "s_waitcnt lgkmcnt(3)\n"
      "v_mfma_f32_16x16x32_bf16 v[18:21], v[30:33], v[80:83], v[18:21]\n"
      "v_mfma_f32_16x16x32_bf16 v[22:25], v[34:37], v[80:83], v[22:25]\n"
      "v_mfma_f32_16x16x32_bf16 v[26:29], v[38:41], v[80:83], v[26:29]\n"
      "ds_read_b128 v[30:33], v62 offset:0\n"
      "ds_read_b128 v[34:37], v62 offset:18432\n"
      "ds_read_b128 v[38:41], v15 offset:36864\n"
      "buffer_load_dwordx4 v[76:79], v0, s[8:11], s39 offen offset:0 sc1\n"
      "s_waitcnt lgkmcnt(3)\n"
      "v_mfma_f32_16x16x32_bf16 v[18:21], v[42:45], v[84:87], v[18:21]\n"
      "v_mfma_f32_16x16x32_bf16 v[22:25], v[46:49], v[84:87], v[22:25]\n"
      "v_mfma_f32_16x16x32_bf16 v[26:29], v[50:53], v[84:87], v[26:29]\n"
      "ds_read_b128 v[42:45], v62 offset:1024\n"
      "ds_read_b128 v[46:49], v62 offset:19456\n"
      "ds_read_b128 v[50:53], v15 offset:37888\n"
      "buffer_load_dwordx4 v[80:83], v0, s[8:11], s39 offen offset:1024 sc1\n"
      "s_waitcnt vmcnt(5)\n"
      "s_waitcnt lgkmcnt(3)\n"
      "v_mfma_f32_16x16x32_bf16 v[18:21], v[30:33], v[88:91], v[18:21]\n"
      "v_mfma_f32_16x16x32_bf16 v[22:25], v[34:37], v[88:91], v[22:25]\n"
      "v_mfma_f32_16x16x32_bf16 v[26:29], v[38:41], v[88:91], v[26:29]\n"
      "ds_read_b128 v[30:33], v62 offset:2048\n"
      "ds_read_b128 v[34:37], v62 offset:20480\n"
      "ds_read_b128 v[38:41], v15 offset:38912\n"
      "buffer_load_dwordx4 v[84:87], v0, s[8:11], s39 offen offset:2048 sc1\n"
      "s_waitcnt lgkmcnt(3)\n"
      "v_mfma_f32_16x16x32_bf16 v[18:21], v[42:45], v[92:95], v[18:21]\n"
      "v_mfma_f32_16x16x32_bf16 v[22:25], v[46:49], v[92:95], v[22:25]\n"
      "v_mfma_f32_16x16x32_bf16 v[26:29], v[50:53], v[92:95], v[26:29]\n"
      "ds_read_b128 v[42:45], v62 offset:3072\n"
      "ds_read_b128 v[46:49], v62 offset:21504\n"
      "ds_read_b128 v[50:53], v15 offset:39936\n"
      "s_waitcnt lgkmcnt(3)\n"
      "v_mfma_f32_16x16x32_bf16 v[18:21], v[30:33], v[96:99], v[18:21]\n"
      "v_mfma_f32_16x16x32_bf16 v[22:25], v[34:37], v[96:99], v[22:25]\n"
      "v_mfma_f32_16x16x32_bf16 v[26:29], v[38:41], v[96:99], v[26:29]\n"
      "ds_read_b128 v[30:33], v62 offset:4096\n"
      "ds_read_b128 v[34:37], v62 offset:22528\n"
      "ds_read_b128 v[38:41], v15 offset:40960\n"
      "s_waitcnt lgkmcnt(3)\n"
      "v_mfma_f32_16x16x32_bf16 v[18:21], v[42:45], v[100:103], v[18:21]\n"
      "v_mfma_f32_16x16x32_bf16 v[22:25], v[46:49], v[100:103], v[22:25]\n"
      "v_mfma_f32_16x16x32_bf16 v[26:29], v[50:53], v[100:103], v[26:29]\n"
      "ds_read_b128 v[42:45], v62 offset:5120\n"
      "ds_read_b128 v[46:49], v62 offset:23552\n"
      "ds_read_b128 v[50:53], v15 offset:41984\n"
      "s_waitcnt lgkmcnt(3)\n"
      "v_mfma_f32_16x16x32_bf16 v[18:21], v[30:33], v[104:107], v[18:21]\n"
      "v_mfma_f32_16x16x32_bf16 v[22:25], v[34:37], v[104:107], v[22:25]\n"
      "v_mfma_f32_16x16x32_bf16 v[26:29], v[38:41], v[104:107], v[26:29]\n"
      "ds_read_b128 v[30:33], v62 offset:6144\n"
      "ds_read_b128 v[34:37], v62 offset:24576\n"
      "ds_read_b128 v[38:41], v15 offset:43008\n"
      "s_waitcnt lgkmcnt(3)\n"
      "v_mfma_f32_16x16x32_bf16 v[18:21], v[42:45], v[108:111], v[18:21]\n"
      "v_mfma_f32_16x16x32_bf16 v[22:25], v[46:49], v[108:111], v[22:25]\n"
      "v_mfma_f32_16x16x32_bf16 v[26:29], v[50:53], v[108:111], v[26:29]\n"
      "ds_read_b128 v[42:45], v62 offset:7168\n"
      "ds_read_b128 v[46:49], v62 offset:25600\n"
      "ds_read_b128 v[50:53], v15 offset:44032\n"
      "s_waitcnt vmcnt(0)\n"
      "s_waitcnt lgkmcnt(3)\n"
      "v_mfma_f32_16x16x32_bf16 v[18:21], v[30:33], v[64:67], v[18:21]\n"
      "v_mfma_f32_16x16x32_bf16 v[22:25], v[34:37], v[64:67], v[22:25]\n"
      "v_mfma_f32_16x16x32_bf16 v[26:29], v[38:41], v[64:67], v[26:29]\n"
      "ds_read_b128 v[30:33], v62 offset:8192\n"
      "ds_read_b128 v[34:37], v62 offset:26624\n"
      "ds_read_b128 v[38:41], v15 offset:45056\n"
      "s_waitcnt lgkmcnt(3)\n"
      "v_mfma_f32_16x16x32_bf16 v[18:21], v[42:45], v[68:71], v[18:21]\n"
      "v_mfma_f32_16x16x32_bf16 v[22:25], v[46:49], v[68:71], v[22:25]\n"
      "v_mfma_f32_16x16x32_bf16 v[26:29], v[50:53], v[68:71], v[26:29]\n"
      "ds_read_b128 v[42:45], v62 offset:9216\n"
      "ds_read_b128 v[46:49], v62 offset:27648\n"
      "ds_read_b128 v[50:53], v15 offset:46080\n"
      "s_waitcnt lgkmcnt(3)\n"
      "v_mfma_f32_16x16x32_bf16 v[18:21], v[30:33], v[72:75], v[18:21]\n"
      "v_mfma_f32_16x16x32_bf16 v[22:25], v[34:37], v[72:75], v[22:25]\n"
      "v_mfma_f32_16x16x32_bf16 v[26:29], v[38:41], v[72:75], v[26:29]\n"
      "ds_read_b128 v[30:33], v62 offset:10240\n"
      "ds_read_b128 v[34:37], v62 offset:28672\n"
      "ds_read_b128 v[38:41], v15 offset:47104\n"
      "s_waitcnt lgkmcnt(3)\n"
      "v_mfma_f32_16x16x32_bf16 v[18:21], v[42:45], v[76:79], v[18:21]\n"
      "v_mfma_f32_16x16x32_bf16 v[22:25], v[46:49], v[76:79], v[22:25]\n"
      "v_mfma_f32_16x16x32_bf16 v[26:29], v[50:53], v[76:79], v[26:29]\n"
      "ds_read_b128 v[42:45], v62 offset:11264\n"
      "ds_read_b128 v[46:49], v62 offset:29696\n"
      "ds_read_b128 v[50:53], v15 offset:48128\n"
      "s_waitcnt lgkmcnt(3)\n"
      "v_mfma_f32_16x16x32_bf16 v[18:21], v[30:33], v[80:83], v[18:21]\n"
      "v_mfma_f32_16x16x32_bf16 v[22:25], v[34:37], v[80:83], v[22:25]\n"
      "v_mfma_f32_16x16x32_bf16 v[26:29], v[38:41], v[80:83], v[26:29]\n"
      "s_waitcnt lgkmcnt(0)\n"
      "v_mfma_f32_16x16x32_bf16 v[18:21], v[42:45], v[84:87], v[18:21]\n"
      "v_mfma_f32_16x16x32_bf16 v[22:25], v[46:49], v[84:87], v[22:25]\n"
      "v_mfma_f32_16x16x32_bf16 v[26:29], v[50:53], v[84:87], v[26:29]\n"
      "s_branch O_EPI_%=\n"
      "O_OUT_%=:\n"
      "s_and_b32 s28, s19, 7\n"
      "s_lshl_b32 s28, s28, 3\n"
      "s_add_u32 s28, s28, s40\n"
      "s_lshl_b32 s28, s28, 5\n"
      "s_lshr_b32 s29, s19, 3\n"
      "s_add_u32 s28, s28, s29\n"
      "s_mul_i32 s28, s28, 1152\n"
      "v_lshrrev_b32 v36, 4, v0\n"
      "v_and_b32 v37, 15, v36\n"
      "v_lshrrev_b32 v36, 4, v36\n"
      "v_mul_u32_u24 v37, 2304, v37\n"
      "v_lshl_add_u32 v36, v36, 3, v37\n"
      "s_lshl_b32 s29, s18, 6\n"
      "s_add_u32 s29, s29, s28\n"
      "buffer_store_dwordx2 v[30:31], v36, s[12:15], s29 offen\n"
      "buffer_store_dwordx2 v[32:33], v36, s[12:15], s29 offen offset:32\n"
      "s_cmp_lg_u32 s88, 3\n"
      "s_cbranch_scc1 O_FIN_%=\n"
      "s_and_b32 s29, s18, 3\n"
      "s_lshl_b32 s29, s29, 5\n"
      "s_add_u32 s29, s29, s28\n"
      "buffer_store_dwordx2 v[34:35], v36, s[12:15], s29 offen offset:1024\n"
      "s_branch O_FIN_%=\n"
      "L_NEXT_%=:\n"
      "s_add_u32 s16, s16, 1\n"
      "s_add_u32 s20, s20, 376832\n"
      "v_add_u32 v1, 32, v1\n"
      "v_add_u32 v2, 32, v2\n"
      "s_cmp_lg_u32 s16, 4\n"
      "s_cbranch_scc1 LAYER_%=\n"
      "s_waitcnt vmcnt(0) lgkmcnt(0)\n"
      :
      : "s"((unsigned)pw), "s"((unsigned)(pw >> 32)), "s"((unsigned)po), "s"((unsigned)(po >> 32)), "s"((unsigned)blockIdx.x), "s"(wave)
      : "v0", "v1", "v2", "v3", "v4", "v5", "v6", "v7", "v8", "v9", "v10", "v11", "v12", "v13", "v14", "v15", "v16", "v17", "v18", "v19", "v20", "v21", "v22", "v23", "v24", "v25", "v26", "v27", "v28", "v29", "v30", "v31", "v32", "v33", "v34", "v35", "v36", "v37", "v38", "v39", "v40", "v41", "v42", "v43", "v44", "v45", "v46", "v47", "v48", "v49", "v50", "v51", "v52", "v53", "v54", "v55", "v56", "v57", "v58", "v59", "v60", "v61", "v62", "v63", "v64", "v65", "v66", "v67", "v68", "v69", "v70", "v71", "v72", "v73", "v74", "v75", "v76", "v77", "v78", "v79", "v80", "v81", "v82", "v83", "v84", "v85", "v86", "v87", "v88", "v89", "v90", "v91", "v92", "v93", "v94", "v95", "v96", "v97", "v98", "v99", "v100", "v101", "v102", "v103", "v104", "v105", "v106", "v107", "v108", "v109", "v110", "v111", "v112", "v113", "v114", "v115", "v116", "v117", "v118", "v119", "v120", "v121", "v122", "v123", "v124", "v125", "v126", "v127", "v128", "v129", "v130", "v131", "v132", "v133", "v134", "v135", "v136", "v137", "v138", "v139", "v140", "v141", "v142", "v143", "v144", "v145", "v146", "v147", "v148", "v149", "v150", "v151", "v152", "v153", "v154", "v155", "v156", "v157", "v158", "v159", "v160", "v161", "v162", "v163", "v164", "v165", "v166", "v167", "v168", "v169", "v170", "v171", "v172", "v173", "v174", "v175", "v176", "v177", "v178", "v179", "v180", "v181", "v182", "v183", "v184", "v185", "v186", "v187", "v188", "v189", "v190", "v191", "v192", "v193", "v194", "v195", "v196", "v197", "v198", "v199", "v200", "v201", "v202", "v203", "v204", "v205", "v206", "v207", "v208", "v209", "v210", "v211", "v212", "v213", "v214", "v215", "v216", "v217", "v218", "v219", "v220", "v221", "v222", "v223", "v224", "v225", "v226", "v227", "v228", "v229", "v230", "v231", "v232", "v233", "v234", "v235", "v236", "v237", "v238", "v239", "v240", "v241", "v242", "v243", "v244", "v245", "v246", "v247", "v248", "v249", "v250", "v251", "v252", "v253", "v254", "v255", "s8", "s9", "s10", "s11", "s12", "s13", "s14", "s15", "s16", "s17", "s18", "s19", "s20", "s21", "s22", "s23", "s24", "s25", "s26", "s27", "s28", "s29", "s30", "s31", "s32", "s33", "s34", "s35", "s36", "s37", "s38", "s39", "s40", "s41", "s42", "s43", "s44", "s45", "s46", "s47", "s48", "s49", "s50", "s51", "s52", "s53", "s54", "s55", "s56", "s57", "s58", "s59", "s60", "s61", "s62", "s63", "s64", "s65", "s66", "s67", "s68", "s69", "s70", "s71", "s72", "s73", "s74", "s75", "s76", "s77", "s78", "s79", "s80", "s81", "s82", "s83", "s84", "s85", "s86", "s87", "s88", "s89", "s90", "s91", "s92", "s93", "s94", "s95", "s96", "s97", "s98", "s99", "vcc", "scc", "memory");
}

extern "C" void kernel_launch(void* const* d_in, const int* in_sizes, int n_in, void* d_out, int out_size, void* d_ws, size_t ws_size,
                              hipStream_t stream) {
  (void)in_sizes; (void)n_in; (void)out_size; (void)ws_size;
  (void)hipFuncSetAttribute((const void*)main_kernel, hipFuncAttributeMaxDynamicSharedMemorySize, LDS_BYTES);
  prep_kernel<<<(NCH_ALL + 255) / 256, 256, 0, stream>>>((const uint16_t*)d_in[0], (const uint16_t*)d_in[1], (const uint16_t*)d_in[2],
                                                          (const uint16_t*)d_in[4], (const uint16_t*)d_in[5], (char*)d_ws);
  main_kernel<<<256, 512, LDS_BYTES, stream>>>((uint16_t*)d_out, (char*)d_ws);
}
```
